```python
import torch

def _softmax(x, axis=-1):
    return torch.softmax(x, dim=axis)

def _gelu(x):
    return torch.nn.functional.gelu(x, approximate='tanh')

def _silu(x):
    return torch.nn.functional.silu(x)

def _sigmoid(x):
    return torch.sigmoid(x)

def _layernorm(x, axis=-1, eps=1e-05):
    m = x.mean(dim=axis, keepdim=True)
    v = x.var(dim=axis, keepdim=True, unbiased=False)
    return (x - m) / torch.sqrt(v + eps)

def _scatter_add(tgt, idx, src, axis=0):
    return tgt.clone().index_add_(axis, idx, src)

def _segment_sum(src, seg, n):
    out = torch.zeros((n,) + src.shape[1:], dtype=src.dtype)
    return out.index_add_(0, seg, src)

def _pad(x, pw):
    flat = tuple((v for pair in reversed(pw) for v in pair))
    return torch.nn.functional.pad(x, flat)
kernel_name = 'parallel_sum__softmax_full_mha__mixer_gmlp__prompt'

def setup_inputs():

    _w = lambda a: (lambda t: t.to(torch.bfloat16) if t.is_floating_point() else t)(torch.as_tensor(a))

    def _A_setup():
        B, S, D = (1, 2048, 512)
        return {'x': _w((lambda *s: torch.randn(*s, dtype=torch.bfloat16) * 0.05)(B, S, D)), 'wqkv': _w((lambda *s: torch.randn(*s, dtype=torch.bfloat16) * 0.2)(D, 3 * D)), 'wo': _w((lambda *s: torch.randn(*s, dtype=torch.bfloat16) * 0.2)(D, D))}
    _A_in = _A_setup()

    def _B_setup():
        B, S, D, F = (1, 2048, 512, 1792)
        Fh = F // 2
        return {'x': _w((lambda *s: torch.randn(*s, dtype=torch.bfloat16) * 0.05)(B, S, D)), 'w_in': _w((lambda *s: torch.randn(*s, dtype=torch.bfloat16) * 0.8)(D, 2 * Fh)), 'w_sp': _w(0.01 * (lambda *s: torch.randn(*s, dtype=torch.bfloat16) * 0.8)(S, S)), 'b_sp': _w(1.0 + 0.01 * (lambda *s: torch.randn(*s, dtype=torch.bfloat16) * 0.2)(S)), 'w_out': _w((lambda *s: torch.randn(*s, dtype=torch.bfloat16) * 0.2)(Fh, D))}
    _B_in = _B_setup()
    return {'A_x': _A_in['x'], 'A_wqkv': _A_in['wqkv'], 'A_wo': _A_in['wo'], 'B_x': _B_in['x'], 'B_w_in': _B_in['w_in'], 'B_w_sp': _B_in['w_sp'], 'B_b_sp': _B_in['b_sp'], 'B_w_out': _B_in['w_out']}

def reference(A_x, A_wqkv, A_wo, B_x, B_w_in, B_w_sp, B_b_sp, B_w_out):

    def _A_ref(x, wqkv, wo):
        B, S, D = x.shape
        H = 32
        Dh = D // H
        qkv = (lambda x, *d: x.permute(*d))((x @ wqkv).reshape(B, S, 3, H, Dh), 2, 0, 3, 1, 4)
        q, k, v = (qkv[0], qkv[1], qkv[2])
        scale = Dh ** (-0.5)
        scores = torch.einsum('bhqd,bhkd->bhqk', q, k) * scale
        p = _softmax(scores, axis=-1)
        o = torch.einsum('bhqk,bhkd->bhqd', p, v)
        return (lambda x, *d: x.permute(*d))(o, 0, 2, 1, 3).reshape(B, S, D) @ wo

    def _B_ref(x, w_in, w_sp, b_sp, w_out):
        B, S, D = x.shape
        F = w_in.shape[-1]
        Fh = F // 2
        z = _gelu(torch.einsum('bsd,df->bsf', x, w_in))
        z1 = z[..., :Fh].clone()
        z2 = _layernorm(z[..., Fh:], axis=-1)
        g = torch.einsum('ts,bsf->btf', w_sp, z2) + b_sp[None, :, None]
        return torch.einsum('bsf,fd->bsd', z1 * g, w_out)
    x = A_x
    xn = _layernorm(x, axis=-1)
    return x + _A_ref(xn, A_wqkv, A_wo) + _B_ref(xn, B_w_in, B_w_sp, B_b_sp, B_w_out)
```

```cpp
#include <hip/hip_runtime.h>
#include <hip/hip_bf16.h>
typedef __hip_bfloat16 bf16;
#define S 2048
#define D 512
#define FH 896
__device__ __forceinline__ float b2f(bf16 v){return __bfloat162float(v);}
__device__ __forceinline__ bf16 f2b(float v){return __float2bfloat16(v);}

__global__ void ln_k(const bf16* x, bf16* xn, int n, int ld_in, int ld_out){
  int row=blockIdx.x; __shared__ float red[256];
  float s=0; for(int i=threadIdx.x;i<n;i+=256) s+=b2f(x[(size_t)row*ld_in+i]);
  red[threadIdx.x]=s; __syncthreads();
  for(int o=128;o>0;o>>=1){ if(threadIdx.x<o) red[threadIdx.x]+=red[threadIdx.x+o]; __syncthreads(); }
  float m=red[0]/n; __syncthreads();
  float v=0; for(int i=threadIdx.x;i<n;i+=256){ float d=b2f(x[(size_t)row*ld_in+i])-m; v+=d*d; }
  red[threadIdx.x]=v; __syncthreads();
  for(int o=128;o>0;o>>=1){ if(threadIdx.x<o) red[threadIdx.x]+=red[threadIdx.x+o]; __syncthreads(); }
  float r=rsqrtf(red[0]/n+1e-5f);
  for(int i=threadIdx.x;i<n;i+=256) xn[(size_t)row*ld_out+i]=f2b((b2f(x[(size_t)row*ld_in+i])-m)*r);
}
__global__ void gemm_k(const bf16* A,int lda,const bf16* B,int ldb,bf16* C,int ldc,int M,int N,int K,int mode,
                       const bf16* z1,int ldz,const bf16* bsp){
  __shared__ float As[16][17], Bs[16][17];
  int tx=threadIdx.x, ty=threadIdx.y; int row=blockIdx.y*16+ty, col=blockIdx.x*16+tx;
  float acc=0;
  for(int k0=0;k0<K;k0+=16){
    As[ty][tx]=b2f(A[(size_t)row*lda+k0+tx]);
    Bs[ty][tx]=b2f(B[(size_t)(k0+ty)*ldb+col]);
    __syncthreads();
    for(int k=0;k<16;k++) acc+=As[ty][k]*Bs[k][tx];
    __syncthreads();
  }
  if(mode==1){ float u=acc; float t=tanhf(0.7978845608028654f*(u+0.044715f*u*u*u)); acc=0.5f*u*(1.f+t);}
  if(mode==2){ acc=(acc+b2f(bsp[row]))*b2f(z1[(size_t)row*ldz+col]); }
  if(mode==3){ acc+=b2f(z1[(size_t)row*ldz+col]); }
  C[(size_t)row*ldc+col]=f2b(acc);
}
__global__ void attn_k(const bf16* qkv, bf16* o, int ldo){
  int h=blockIdx.y; int q=blockIdx.x*64+threadIdx.x;
  float qv[16]; for(int d=0;d<16;d++) qv[d]=b2f(qkv[(size_t)q*1536+h*16+d])*0.25f;
  float m=-1e30f,l=0,acc[16]; for(int d=0;d<16;d++) acc[d]=0;
  for(int k=0;k<S;k++){
    float s=0; for(int d=0;d<16;d++) s+=qv[d]*b2f(qkv[(size_t)k*1536+512+h*16+d]);
    float mn=fmaxf(m,s); float a=__expf(m-mn), p=__expf(s-mn);
    l=l*a+p; for(int d=0;d<16;d++) acc[d]=acc[d]*a+p*b2f(qkv[(size_t)k*1536+1024+h*16+d]);
    m=mn;
  }
  for(int d=0;d<16;d++) o[(size_t)q*ldo+h*16+d]=f2b(acc[d]/l);
}
extern "C" void kernel_launch(void* const* d_in, const int* in_sizes, int n_in,
                              void* d_out, int out_size, void* d_ws, size_t ws_size,
                              hipStream_t stream) {
  const bf16* x=(const bf16*)d_in[0]; const bf16* wqkv=(const bf16*)d_in[1]; const bf16* wo=(const bf16*)d_in[2];
  const bf16* w_in=(const bf16*)d_in[4]; const bf16* w_sp=(const bf16*)d_in[5]; const bf16* b_sp=(const bf16*)d_in[6]; const bf16* w_out=(const bf16*)d_in[7];
  bf16* out=(bf16*)d_out;
  char* ws=(char*)d_ws;
  bf16* xn=(bf16*)ws; ws+=S*D*2;
  bf16* qkv=(bf16*)ws; ws+=S*1536*2;
  bf16* z=(bf16*)ws; ws+=S*1792*2;
  bf16* z2n=(bf16*)ws; ws+=S*FH*2;
  bf16* ou=(bf16*)ws; ws+=S*1408*2;
  bf16* wcat=(bf16*)ws; ws+=1408*512*2;
  ln_k<<<S,256,0,stream>>>(x,xn,D,D,D);
  dim3 b(16,16);
  gemm_k<<<dim3(1536/16,S/16),b,0,stream>>>(xn,D,wqkv,1536,qkv,1536,S,1536,D,0,nullptr,0,nullptr);
  gemm_k<<<dim3(1792/16,S/16),b,0,stream>>>(xn,D,w_in,1792,z,1792,S,1792,D,1,nullptr,0,nullptr);
  ln_k<<<S,256,0,stream>>>(z+FH,z2n,FH,1792,FH);
  attn_k<<<dim3(S/64,32),64,0,stream>>>(qkv,ou,1408);
  gemm_k<<<dim3(FH/16,S/16),b,0,stream>>>(w_sp,S,z2n,FH,ou+512,1408,S,FH,S,2,z,1792,b_sp);
  hipMemcpyAsync(wcat,wo,512*512*2,hipMemcpyDeviceToDevice,stream);
  hipMemcpyAsync(wcat+512*512,w_out,FH*512*2,hipMemcpyDeviceToDevice,stream);
  gemm_k<<<dim3(512/16,S/16),b,0,stream>>>(ou,1408,wcat,512,out,512,S,512,1408,3,x,512,nullptr);
}
```

```cpp
#include <hip/hip_runtime.h>
#include <hip/hip_bf16.h>
#include <stdint.h>

typedef __attribute__((ext_vector_type(16))) float f32x16;
typedef __attribute__((ext_vector_type(4))) float f32x4;
typedef __attribute__((ext_vector_type(2))) float f32x2;
typedef __attribute__((ext_vector_type(4))) unsigned u32x4;
typedef __attribute__((ext_vector_type(2))) unsigned u32x2;
typedef __attribute__((ext_vector_type(2))) __bf16 bf2;
typedef __attribute__((ext_vector_type(8))) __bf16 bfv8;
typedef __attribute__((ext_vector_type(4))) short s16x4;
#define LAS __attribute__((address_space(3)))
#define GAS __attribute__((address_space(1)))
#if defined(__HIP_DEVICE_COMPILE__)
#define NOPK __attribute__((target("no-packed-fp32-ops")))
#else
#define NOPK
#endif
#define DI NOPK __device__ __forceinline__
#define LAM NOPK __attribute__((always_inline))
#define U2F(x) __builtin_bit_cast(float, (unsigned)(x))
#define F2U(x) __builtin_bit_cast(unsigned, (float)(x))
#define WALL(p) (__builtin_amdgcn_ballot_w64(!(p)) == 0ull)
#define WANY(p) (__builtin_amdgcn_ballot_w64(p) != 0ull)
#define TIDX() ((int)__builtin_amdgcn_workitem_id_x())
#define BIDX() ((int)__builtin_amdgcn_workgroup_id_x())
#define GSTAMP(i)
#define ATT_FORCE_FALLBACK 0
#define MFMA32(a, b, c) __builtin_amdgcn_mfma_f32_32x32x16_bf16(__builtin_bit_cast(bfv8, a), __builtin_bit_cast(bfv8, b), c, 0, 0, 0)

constexpr size_t WS_QF = 0;
constexpr size_t WS_KV = WS_QF + (2u << 20);
constexpr size_t WS_XN = WS_KV + (4u << 20);
constexpr size_t WS_WIN = WS_XN + (2u << 20);
constexpr size_t WS_WC = WS_WIN + 1792u * 1024;
constexpr size_t WS_Z2 = WS_WC + 1408u * 1024;
constexpr size_t WS_AF = WS_Z2 + 3584u * 1024;
constexpr size_t WS_FL = WS_AF + 5632u * 1024;
constexpr size_t FL_BYTES = 512u * 1024;

DI unsigned cvtpk(float lo, float hi) { f32x2 t = {lo, hi}; return __builtin_bit_cast(unsigned, __builtin_convertvector(t, bf2)); }
DI float bflo(unsigned v) { return U2F(v << 16); }
DI float bfhi(unsigned v) { return U2F(v & 0xffff0000u); }
DI float dot2(unsigned a, unsigned b, float c) { return __builtin_amdgcn_fdot2_f32_bf16(__builtin_bit_cast(bf2, a), __builtin_bit_cast(bf2, b), c, false); }
DI float xhalf_add(float v) {
  auto r = __builtin_amdgcn_permlane32_swap(F2U(v), F2U(v), false, false);
  return U2F(r[0]) + U2F(r[1]);
}
DI void glds1(const void* sbase, unsigned voff, unsigned ldsb) {
  unsigned sv;
  asm volatile("s_mov_b32 %0, m0\n\ts_mov_b32 m0, %3\n\ts_nop 0\n\tglobal_load_lds_dwordx4 %1, %2\n\ts_mov_b32 m0, %0"
               : "=&s"(sv) : "v"(voff), "s"(sbase), "s"(ldsb) : "memory");
}
DI void glds2(const void* sbase, unsigned voff, unsigned ldsb) {
  unsigned sv;
  asm volatile("s_mov_b32 %0, m0\n\ts_mov_b32 m0, %3\n\ts_nop 0\n\tglobal_load_lds_dwordx4 %1, %2\n\tglobal_load_lds_dwordx4 %1, %2 offset:1024\n\ts_mov_b32 m0, %0"
               : "=&s"(sv) : "v"(voff), "s"(sbase), "s"(ldsb) : "memory");
}
template <int N> DI void wait_bar() { asm volatile("s_waitcnt vmcnt(%0) lgkmcnt(0)\n\ts_barrier" ::"i"(N) : "memory"); }
DI void lds_wait() { asm volatile("" ::: "memory"); __builtin_amdgcn_s_waitcnt(0xC07F); asm volatile("" ::: "memory"); }
DI void raw_bar() { asm volatile("s_barrier" ::: "memory"); }
DI void lds_bar() { lds_wait(); raw_bar(); }
DI u32x2 trread(const LAS char* p) { return __builtin_bit_cast(u32x2, __builtin_amdgcn_ds_read_tr16_b64_v4i16((LAS s16x4*)p)); }
DI u32x4 trfrag(const LAS char* p, int o0, int o1) { u32x2 a = trread(p + o0), b = trread(p + o1); u32x4 r = {a[0], a[1], b[0], b[1]}; return r; }
#define ATTN_LOOP_ASM \
  "s_mov_b32 %[sm0], m0\n\t" \
  "ds_read_b128 v[104:107], %[kl]\n\t" \
  "ds_read_b128 v[108:111], %[vb]\n\t" \
  "ds_read_b128 v[112:115], %[vb] offset:512\n\t" \
  "v_mov_b32 v100, 0\n\t" \
  "v_mov_b32 v101, 0\n\t" \
  "v_mov_b32 v102, 0\n\t" \
  "v_mov_b32 v103, 0\n\t" \
  "v_mov_b32 v88, 0\n\t" \
  "v_mov_b32 v89, 0\n\t" \
  "v_mov_b32 v90, 0\n\t" \
  "v_mov_b32 v91, 0\n\t" \
  "v_mov_b32 v92, 0\n\t" \
  "v_mov_b32 v93, 0\n\t" \
  "v_mov_b32 v94, 0\n\t" \
  "v_mov_b32 v95, 0\n\t" \
  "v_mov_b32 v120, 0\n\t" \
  "v_mov_b32 v121, 0\n\t" \
  "v_mov_b32 v122, 0\n\t" \
  "v_mov_b32 v123, 0\n\t" \
  "v_mov_b32 v124, %[kl]\n\t" \
  "v_mov_b32 v126, %[vb]\n\t" \
  "s_mov_b32 %[c], 0\n\t" \
  "s_mov_b32 %[sc], 0\n\t" \
  "s_waitcnt lgkmcnt(2)\n\t" \
  "v_mfma_f32_32x32x16_bf16 v[64:79], v[104:107], %[q], %[negm]\n\t" \
  "ds_read_b128 v[104:107], %[kl] offset:1024\n\t" \
  "s_nop 10\n\t" \
  ".Lloop%=:\n\t" \
  "s_waitcnt lgkmcnt(0)\n\t" \
  "v_exp_f32 v64, v64\n\t" \
  "v_exp_f32 v65, v65\n\t" \
  "v_cvt_pk_bf16_f32 v103, v94, v95\n\t" \
  "v_mfma_f32_32x32x16_bf16 v[80:95], v[104:107], %[q], %[negm]\n\t" \
  "ds_read_b128 v[104:107], v124 offset:2048\n\t" \
  "v_exp_f32 v66, v66\n\t" \
  "ds_read_b128 v[116:119], v126 offset:1024\n\t" \
  "v_exp_f32 v67, v67\n\t" \
  "s_setprio 1\n\t" \
  "v_cvt_pk_bf16_f32 v96, v64, v65\n\t" \
  "v_exp_f32 v68, v68\n\t" \
  "v_exp_f32 v69, v69\n\t" \
  "s_xor_b32 %[t0], %[sc], 0x8000\n\t" \
  "v_cvt_pk_bf16_f32 v97, v66, v67\n\t" \
  "v_mfma_f32_32x32x16_bf16 %[o], v[120:123], v[100:103], %[o]\n\t" \
  "ds_read_b128 v[120:123], v126 offset:1536\n\t" \
  "v_exp_f32 v70, v70\n\t" \
  "v_exp_f32 v71, v71\n\t" \
  "s_add_u32 m0, %[t0], %[mb]\n\t" \
  "v_cvt_pk_bf16_f32 v98, v68, v69\n\t" \
  "v_exp_f32 v72, v72\n\t" \
  "v_exp_f32 v73, v73\n\t" \
  "v_cvt_pk_bf16_f32 v99, v70, v71\n\t" \
  "v_exp_f32 v74, v74\n\t" \
  "v_exp_f32 v75, v75\n\t" \
  "v_cvt_pk_bf16_f32 v100, v72, v73\n\t" \
  "v_mfma_f32_32x32x16_bf16 %[o], v[108:111], v[96:99], %[o]\n\t" \
  "v_exp_f32 v76, v76\n\t" \
  "v_exp_f32 v77, v77\n\t" \
  "v_cvt_pk_bf16_f32 v101, v74, v75\n\t" \
  "v_exp_f32 v78, v78\n\t" \
  "v_exp_f32 v79, v79\n\t" \
  "v_cvt_pk_bf16_f32 v102, v76, v77\n\t" \
  "s_waitcnt lgkmcnt(0)\n\t" \
  "v_exp_f32 v80, v80\n\t" \
  "v_exp_f32 v81, v81\n\t" \
  "v_cvt_pk_bf16_f32 v103, v78, v79\n\t" \
  "v_mfma_f32_32x32x16_bf16 v[64:79], v[104:107], %[q], %[negm]\n\t" \
  "ds_read_b128 v[104:107], v124 offset:3072\n\t" \
  "v_exp_f32 v82, v82\n\t" \
  "ds_read_b128 v[108:111], v126 offset:2048\n\t" \
  "v_exp_f32 v83, v83\n\t" \
  "v_cvt_pk_bf16_f32 v96, v80, v81\n\t" \
  "v_exp_f32 v84, v84\n\t" \
  "v_exp_f32 v85, v85\n\t" \
  "v_cvt_pk_bf16_f32 v97, v82, v83\n\t" \
  "v_mfma_f32_32x32x16_bf16 %[o], v[112:115], v[100:103], %[o]\n\t" \
  "ds_read_b128 v[112:115], v126 offset:2560\n\t" \
  "v_exp_f32 v86, v86\n\t" \
  "v_exp_f32 v87, v87\n\t" \
  "v_cvt_pk_bf16_f32 v98, v84, v85\n\t" \
  "v_exp_f32 v88, v88\n\t" \
  "v_exp_f32 v89, v89\n\t" \
  "global_load_lds_dwordx4 %[goff], %[gb]\n\t" \
  "v_cvt_pk_bf16_f32 v99, v86, v87\n\t" \
  "v_exp_f32 v90, v90\n\t" \
  "v_exp_f32 v91, v91\n\t" \
  "v_cvt_pk_bf16_f32 v100, v88, v89\n\t" \
  "v_mfma_f32_32x32x16_bf16 %[o], v[116:119], v[96:99], %[o]\n\t" \
  "v_exp_f32 v92, v92\n\t" \
  "v_exp_f32 v93, v93\n\t" \
  "v_cvt_pk_bf16_f32 v101, v90, v91\n\t" \
  "v_exp_f32 v94, v94\n\t" \
  "v_exp_f32 v95, v95\n\t" \
  "v_cvt_pk_bf16_f32 v102, v92, v93\n\t" \
  "s_waitcnt lgkmcnt(0)\n\t" \
  "v_exp_f32 v64, v64\n\t" \
  "v_exp_f32 v65, v65\n\t" \
  "v_cvt_pk_bf16_f32 v103, v94, v95\n\t" \
  "v_mfma_f32_32x32x16_bf16 v[80:95], v[104:107], %[q], %[negm]\n\t" \
  "ds_read_b128 v[104:107], v124 offset:4096\n\t" \
  "v_exp_f32 v66, v66\n\t" \
  "ds_read_b128 v[116:119], v126 offset:3072\n\t" \
  "v_exp_f32 v67, v67\n\t" \
  "s_setprio 1\n\t" \
  "v_cvt_pk_bf16_f32 v96, v64, v65\n\t" \
  "v_exp_f32 v68, v68\n\t" \
  "v_exp_f32 v69, v69\n\t" \
  "v_cvt_pk_bf16_f32 v97, v66, v67\n\t" \
  "v_mfma_f32_32x32x16_bf16 %[o], v[120:123], v[100:103], %[o]\n\t" \
  "ds_read_b128 v[120:123], v126 offset:3584\n\t" \
  "v_exp_f32 v70, v70\n\t" \
  "v_exp_f32 v71, v71\n\t" \
  "v_cvt_pk_bf16_f32 v98, v68, v69\n\t" \
  "v_exp_f32 v72, v72\n\t" \
  "v_exp_f32 v73, v73\n\t" \
  "v_cvt_pk_bf16_f32 v99, v70, v71\n\t" \
  "v_exp_f32 v74, v74\n\t" \
  "v_exp_f32 v75, v75\n\t" \
  "v_cvt_pk_bf16_f32 v100, v72, v73\n\t" \
  "v_mfma_f32_32x32x16_bf16 %[o], v[108:111], v[96:99], %[o]\n\t" \
  "v_exp_f32 v76, v76\n\t" \
  "v_exp_f32 v77, v77\n\t" \
  "v_cvt_pk_bf16_f32 v101, v74, v75\n\t" \
  "v_exp_f32 v78, v78\n\t" \
  "v_exp_f32 v79, v79\n\t" \
  "v_cvt_pk_bf16_f32 v102, v76, v77\n\t" \
  "s_waitcnt lgkmcnt(0)\n\t" \
  "v_exp_f32 v80, v80\n\t" \
  "v_exp_f32 v81, v81\n\t" \
  "v_cvt_pk_bf16_f32 v103, v78, v79\n\t" \
  "v_mfma_f32_32x32x16_bf16 v[64:79], v[104:107], %[q], %[negm]\n\t" \
  "ds_read_b128 v[104:107], v124 offset:5120\n\t" \
  "v_exp_f32 v82, v82\n\t" \
  "ds_read_b128 v[108:111], v126 offset:4096\n\t" \
  "v_exp_f32 v83, v83\n\t" \
  "v_cvt_pk_bf16_f32 v96, v80, v81\n\t" \
  "v_exp_f32 v84, v84\n\t" \
  "v_exp_f32 v85, v85\n\t" \
  "v_cvt_pk_bf16_f32 v97, v82, v83\n\t" \
  "v_mfma_f32_32x32x16_bf16 %[o], v[112:115], v[100:103], %[o]\n\t" \
  "ds_read_b128 v[112:115], v126 offset:4608\n\t" \
  "v_exp_f32 v86, v86\n\t" \
  "v_exp_f32 v87, v87\n\t" \
  "v_cvt_pk_bf16_f32 v98, v84, v85\n\t" \
  "v_exp_f32 v88, v88\n\t" \
  "v_exp_f32 v89, v89\n\t" \
  "global_load_lds_dwordx4 %[goff], %[gb] offset:1024\n\t" \
  "v_cvt_pk_bf16_f32 v99, v86, v87\n\t" \
  "v_exp_f32 v90, v90\n\t" \
  "v_exp_f32 v91, v91\n\t" \
  "v_cvt_pk_bf16_f32 v100, v88, v89\n\t" \
  "v_mfma_f32_32x32x16_bf16 %[o], v[116:119], v[96:99], %[o]\n\t" \
  "v_exp_f32 v92, v92\n\t" \
  "v_exp_f32 v93, v93\n\t" \
  "v_cvt_pk_bf16_f32 v101, v90, v91\n\t" \
  "v_exp_f32 v94, v94\n\t" \
  "v_exp_f32 v95, v95\n\t" \
  "v_cvt_pk_bf16_f32 v102, v92, v93\n\t" \
  "s_waitcnt lgkmcnt(0)\n\t" \
  "v_exp_f32 v64, v64\n\t" \
  "v_exp_f32 v65, v65\n\t" \
  "v_cvt_pk_bf16_f32 v103, v94, v95\n\t" \
  "v_mfma_f32_32x32x16_bf16 v[80:95], v[104:107], %[q], %[negm]\n\t" \
  "ds_read_b128 v[104:107], v124 offset:6144\n\t" \
  "v_exp_f32 v66, v66\n\t" \
  "ds_read_b128 v[116:119], v126 offset:5120\n\t" \
  "v_exp_f32 v67, v67\n\t" \
  "s_setprio 0\n\t" \
  "v_cvt_pk_bf16_f32 v96, v64, v65\n\t" \
  "v_exp_f32 v68, v68\n\t" \
  "v_exp_f32 v69, v69\n\t" \
  "s_add_u32 %[sc], %[sc], 0x4000\n\t" \
  "v_cvt_pk_bf16_f32 v97, v66, v67\n\t" \
  "v_mfma_f32_32x32x16_bf16 %[o], v[120:123], v[100:103], %[o]\n\t" \
  "ds_read_b128 v[120:123], v126 offset:5632\n\t" \
  "v_exp_f32 v70, v70\n\t" \
  "v_exp_f32 v71, v71\n\t" \
  "s_and_b32 %[sc], %[sc], 0xc000\n\t" \
  "v_cvt_pk_bf16_f32 v98, v68, v69\n\t" \
  "v_exp_f32 v72, v72\n\t" \
  "v_exp_f32 v73, v73\n\t" \
  "v_add_u32 %[goff], 0x4000, %[goff]\n\t" \
  "v_cvt_pk_bf16_f32 v99, v70, v71\n\t" \
  "v_exp_f32 v74, v74\n\t" \
  "v_exp_f32 v75, v75\n\t" \
  "v_and_b32 %[goff], 0x1ffff, %[goff]\n\t" \
  "v_cvt_pk_bf16_f32 v100, v72, v73\n\t" \
  "v_mfma_f32_32x32x16_bf16 %[o], v[108:111], v[96:99], %[o]\n\t" \
  "v_exp_f32 v76, v76\n\t" \
  "v_exp_f32 v77, v77\n\t" \
  "v_cvt_pk_bf16_f32 v101, v74, v75\n\t" \
  "v_exp_f32 v78, v78\n\t" \
  "v_exp_f32 v79, v79\n\t" \
  "v_cvt_pk_bf16_f32 v102, v76, v77\n\t" \
  "s_waitcnt lgkmcnt(0)\n\t" \
  "v_exp_f32 v80, v80\n\t" \
  "v_exp_f32 v81, v81\n\t" \
  "v_cvt_pk_bf16_f32 v103, v78, v79\n\t" \
  "v_mfma_f32_32x32x16_bf16 v[64:79], v[104:107], %[q], %[negm]\n\t" \
  "ds_read_b128 v[104:107], v124 offset:7168\n\t" \
  "v_exp_f32 v82, v82\n\t" \
  "ds_read_b128 v[108:111], v126 offset:6144\n\t" \
  "v_exp_f32 v83, v83\n\t" \
  "v_cvt_pk_bf16_f32 v96, v80, v81\n\t" \
  "v_exp_f32 v84, v84\n\t" \
  "v_exp_f32 v85, v85\n\t" \
  "v_add_u32 v125, %[sc], %[kl]\n\t" \
  "v_cvt_pk_bf16_f32 v97, v82, v83\n\t" \
  "v_mfma_f32_32x32x16_bf16 %[o], v[112:115], v[100:103], %[o]\n\t" \
  "ds_read_b128 v[112:115], v126 offset:6656\n\t" \
  "v_exp_f32 v86, v86\n\t" \
  "v_exp_f32 v87, v87\n\t" \
  "v_and_b32 v127, %[sc], %[dm]\n\t" \
  "v_cvt_pk_bf16_f32 v98, v84, v85\n\t" \
  "v_exp_f32 v88, v88\n\t" \
  "v_exp_f32 v89, v89\n\t" \
  "v_add_u32 v127, v127, %[vb]\n\t" \
  "v_cvt_pk_bf16_f32 v99, v86, v87\n\t" \
  "v_exp_f32 v90, v90\n\t" \
  "v_exp_f32 v91, v91\n\t" \
  "v_cvt_pk_bf16_f32 v100, v88, v89\n\t" \
  "v_mfma_f32_32x32x16_bf16 %[o], v[116:119], v[96:99], %[o]\n\t" \
  "v_exp_f32 v92, v92\n\t" \
  "v_exp_f32 v93, v93\n\t" \
  "v_cvt_pk_bf16_f32 v101, v90, v91\n\t" \
  "v_exp_f32 v94, v94\n\t" \
  "v_exp_f32 v95, v95\n\t" \
  "v_cvt_pk_bf16_f32 v102, v92, v93\n\t" \
  "s_waitcnt vmcnt(2) lgkmcnt(0)\n\t" \
  "s_barrier\n\t" \
  "v_exp_f32 v64, v64\n\t" \
  "v_exp_f32 v65, v65\n\t" \
  "v_cvt_pk_bf16_f32 v103, v94, v95\n\t" \
  "v_mfma_f32_32x32x16_bf16 v[80:95], v[104:107], %[q], %[negm]\n\t" \
  "ds_read_b128 v[104:107], v125 offset:0\n\t" \
  "v_exp_f32 v66, v66\n\t" \
  "ds_read_b128 v[116:119], v126 offset:7168\n\t" \
  "v_exp_f32 v67, v67\n\t" \
  "s_setprio 2\n\t" \
  "v_cvt_pk_bf16_f32 v96, v64, v65\n\t" \
  "v_exp_f32 v68, v68\n\t" \
  "v_exp_f32 v69, v69\n\t" \
  "v_cvt_pk_bf16_f32 v97, v66, v67\n\t" \
  "v_mfma_f32_32x32x16_bf16 %[o], v[120:123], v[100:103], %[o]\n\t" \
  "ds_read_b128 v[120:123], v126 offset:7680\n\t" \
  "v_exp_f32 v70, v70\n\t" \
  "v_exp_f32 v71, v71\n\t" \
  "v_cvt_pk_bf16_f32 v98, v68, v69\n\t" \
  "v_exp_f32 v72, v72\n\t" \
  "v_exp_f32 v73, v73\n\t" \
  "v_cvt_pk_bf16_f32 v99, v70, v71\n\t" \
  "v_exp_f32 v74, v74\n\t" \
  "v_exp_f32 v75, v75\n\t" \
  "v_cvt_pk_bf16_f32 v100, v72, v73\n\t" \
  "v_mfma_f32_32x32x16_bf16 %[o], v[108:111], v[96:99], %[o]\n\t" \
  "v_exp_f32 v76, v76\n\t" \
  "v_exp_f32 v77, v77\n\t" \
  "v_cvt_pk_bf16_f32 v101, v74, v75\n\t" \
  "v_exp_f32 v78, v78\n\t" \
  "v_exp_f32 v79, v79\n\t" \
  "v_cvt_pk_bf16_f32 v102, v76, v77\n\t" \
  "s_waitcnt lgkmcnt(0)\n\t" \
  "v_exp_f32 v80, v80\n\t" \
  "v_exp_f32 v81, v81\n\t" \
  "v_cvt_pk_bf16_f32 v103, v78, v79\n\t" \
  "v_mfma_f32_32x32x16_bf16 v[64:79], v[104:107], %[q], %[negm]\n\t" \
  "ds_read_b128 v[104:107], v125 offset:1024\n\t" \
  "v_exp_f32 v82, v82\n\t" \
  "ds_read_b128 v[108:111], v127 offset:0\n\t" \
  "v_exp_f32 v83, v83\n\t" \
  "v_cvt_pk_bf16_f32 v96, v80, v81\n\t" \
  "v_exp_f32 v84, v84\n\t" \
  "v_exp_f32 v85, v85\n\t" \
  "v_cvt_pk_bf16_f32 v97, v82, v83\n\t" \
  "v_mfma_f32_32x32x16_bf16 %[o], v[112:115], v[100:103], %[o]\n\t" \
  "ds_read_b128 v[112:115], v127 offset:512\n\t" \
  "v_exp_f32 v86, v86\n\t" \
  "v_exp_f32 v87, v87\n\t" \
  "v_cvt_pk_bf16_f32 v98, v84, v85\n\t" \
  "v_exp_f32 v88, v88\n\t" \
  "v_exp_f32 v89, v89\n\t" \
  "v_mov_b32 v124, v125\n\t" \
  "v_cvt_pk_bf16_f32 v99, v86, v87\n\t" \
  "v_exp_f32 v90, v90\n\t" \
  "v_exp_f32 v91, v91\n\t" \
  "v_mov_b32 v126, v127\n\t" \
  "v_cvt_pk_bf16_f32 v100, v88, v89\n\t" \
  "v_mfma_f32_32x32x16_bf16 %[o], v[116:119], v[96:99], %[o]\n\t" \
  "v_exp_f32 v92, v92\n\t" \
  "v_exp_f32 v93, v93\n\t" \
  "s_add_u32 %[c], %[c], 1\n\t" \
  "v_cvt_pk_bf16_f32 v101, v90, v91\n\t" \
  "v_exp_f32 v94, v94\n\t" \
  "s_cmp_lt_u32 %[c], 8\n\t" \
  "v_exp_f32 v95, v95\n\t" \
  "v_cvt_pk_bf16_f32 v102, v92, v93\n\t" \
  "s_cbranch_scc1 .Lloop%=\n\t" \
  "s_nop 0\n\t" \
  "v_cvt_pk_bf16_f32 v103, v94, v95\n\t" \
  "s_waitcnt lgkmcnt(0)\n\t" \
  "s_nop 1\n\t" \
  "v_mfma_f32_32x32x16_bf16 %[o], v[120:123], v[100:103], %[o]\n\t" \
  "s_mov_b32 m0, %[sm0]\n\t" \
  "s_setprio 0\n\t" \
  "s_nop 15\n\t" \
  ""
#define ATTN_CLOBBERS "v64","v65","v66","v67","v68","v69","v70","v71","v72","v73","v74","v75","v76","v77","v78","v79","v80","v81","v82","v83","v84","v85","v86","v87","v88","v89","v90","v91","v92","v93","v94","v95","v96","v97","v98","v99","v100","v101","v102","v103","v104","v105","v106","v107","v108","v109","v110","v111","v112","v113","v114","v115","v116","v117","v118","v119","v120","v121","v122","v123","v124","v125","v126","v127"
constexpr int K1_SLOT = 32768, K1_SIDE = 4 * K1_SLOT, K1_MISC = K1_SIDE + 16384, K1_LDS = K1_MISC + 9216;
constexpr float QSCALE = 0.25f * 1.4426950408889634f;
#define STAMP(i)
#define STAMP_DECL
#define STAMP_OUT
#define ABL_CS 0
#define ABL_ST 0
#define K1_MAP 0
#define PINV(x) asm volatile("" : "+v"(x))
#define PINS(x) asm volatile("" : "+s"(x))

struct K1Frags { u32x4 a[2], b0[2], b1[2], bv[2]; };

NOPK __global__ __launch_bounds__(768, 3) void k1_kernel(const char* x, const char* wqkv, const char* wo, const char* w_in, const char* w_out, char* ws) {
  extern __shared__ __attribute__((aligned(16))) char lds[];
  STAMP_DECL
  GSTAMP(512 * 16);
  const int wid = __builtin_amdgcn_readfirstlane(TIDX() >> 6);
  const int lane = TIDX() & 63, b = BIDX();
  const int xcd = b & 7, j = b >> 3;
  const int panel = (xcd & 3) * 8 + (j & 7), ct = (xcd >> 2) * 4 + (j >> 3);
  const int rot = panel + ct;
  const int r32 = lane & 31, h2 = lane >> 5;
  const unsigned lds0 = (unsigned)(uintptr_t)lds;
  const LAS char* L = (const LAS char*)lds;
  LAS char* LW = (LAS char*)lds;
  if (__builtin_expect(wid < 4, 1)) {
    const int r8 = lane >> 3, p8 = lane & 7;
    const unsigned xv0 = (wid * 16 + r8) * 1024 + ((p8 ^ (r8 >> 1)) << 4) + 4096 + 4096;
    const unsigned xv1 = (wid * 16 + 8 + r8) * 1024 + ((p8 ^ ((r8 >> 1) + 4)) << 4) + 4096 + 3072;
    const unsigned wv = (wid * 16 + r8) * 3072 + ((p8 ^ ((r8 & 2) << 1)) << 4) + 4096;
    const unsigned w0 = wv + 2048, w1 = wv + 24576 + 1024, w2 = wv + 1024, w3 = wv + 24576 + 1024 - 1024, w4 = wv + 2048 - 2048, w5 = wv + 24576 + 2048 - 3072;
    const char* xp = x + (size_t)panel * 65536 - 4096;
    const char* wp = wqkv + ct * 128 - 4096;
    auto stage = [&](int i) LAM {
      const int s = (i + rot) & 7;
      const char* xs = xp + s * 128; const char* wsrc = wp + s * 196608;
      const unsigned mb = lds0 + (i & 3) * K1_SLOT + wid * 8192 + 4096;
      asm volatile("s_mov_b32 m0, %10\n\ts_nop 0\n\t"
                   "global_load_lds_dwordx4 %0, %8 offset:-4096\n\tglobal_load_lds_dwordx4 %2, %9 offset:-2048\n\tglobal_load_lds_dwordx4 %3, %9 offset:-1024\n\t"
                   "global_load_lds_dwordx4 %1, %8 offset:-3072\n\tglobal_load_lds_dwordx4 %4, %9\n\tglobal_load_lds_dwordx4 %5, %9 offset:1024\n\t"
                   "global_load_lds_dwordx4 %6, %9 offset:2048\n\tglobal_load_lds_dwordx4 %7, %9 offset:3072"
                   :: "v"(xv0), "v"(xv1), "v"(w0), "v"(w1), "v"(w2), "v"(w3), "v"(w4), "v"(w5), "s"(xs), "s"(wsrc), "s"(mb) : "memory");
    };
    stage(0); stage(1);
#pragma unroll 1
    for (int i = 0; i < 6; i++) { wait_bar<8>(); stage(i + 2); }
    const int trb = ((lane & 15) >> 2) * 64 + ((lane >> 4) & 1) * 32 + (lane & 3) * 8;
    int sid[4];
#pragma unroll
    for (int i = 0; i < 4; i++) {
      const int t = wid * 4 + i; int id = b * 13 + t; if (t >= 13 || id >= 3200) id = -1;
      sid[i] = id; if (id < 0) id = 0;
      const char* src; unsigned pitch;
      if (id < 1792) { src = w_in + (size_t)(id / 56) * 16 * 3584 + (id % 56) * 64; pitch = 3584; }
      else { const int u = id - 1792, ks = u >> 4; src = (ks < 32 ? wo + (size_t)ks * 16384 : w_out + (size_t)(ks - 32) * 16384) + (u & 15) * 64; pitch = 1024; }
      const unsigned vo = (lane >> 2) * pitch + (lane & 3) * 16, mb = lds0 + K1_SIDE + t * 1024;
      asm volatile("s_mov_b32 m0, %2\n\ts_nop 0\n\tglobal_load_lds_dwordx4 %0, %1" :: "v"(vo), "s"(src), "s"(mb) : "memory");
      if (i == 1) wait_bar<10>();
    }
    wait_bar<4>();
    if (wid < 2) { u32x4 z = {0u, 0u, 0u, 0u}; *(u32x4*)(ws + WS_FL + b * 2048 + wid * 1024 + lane * 16) = z; }
    raw_bar(); raw_bar();
    asm volatile("s_waitcnt vmcnt(0)" ::: "memory");
#pragma unroll
    for (int i = 0; i < 4; i++) {
      const int id = sid[i];
      const bool nat = id < 1792;
      const u32x4 f = trfrag(L + K1_SIDE + (wid * 4 + i) * 1024 + trb + h2 * (nat ? 512 : 256), 0, nat ? 256 : 512);
      if (id >= 0) {
        char* dst = nat ? ws + WS_WIN + ((size_t)(id % 56) * 32 + id / 56) * 1024 : ws + WS_WC + ((size_t)((id - 1792) & 15) * 88 + ((id - 1792) >> 4)) * 1024;
        *(u32x4*)(dst + lane * 16) = f;
      }
    }
    GSTAMP(512 * 16 + 1);
    return;
  }
  const int cw = wid - 4, mt = cw & 1, nh = (cw >> 1) & 1, kh = cw >> 2;
  const int rr = r32 ^ (kh << 4);
  const int arow = mt * 32 + rr;
  const int ks0 = 2 * kh + nh, ks1 = 2 * kh + (nh ^ 1);
  const int ab = (arow >> 4) * 8192 + (arow & 15) * 128, asw = (arow >> 1) & 7;
  const int aoff0 = ab + (((2 * ks0 + h2) ^ asw) << 4), aoff1 = ab + (((2 * ks1 + h2) ^ asw) << 4);
  const int q4 = (lane & 15) >> 2;
  const int trw = 2048 + (8 * h2 + q4) * 128 + ((nh ^ (q4 >> 1)) << 6) + ((lane >> 4) & 1) * 32 + (lane & 3) * 8;
  const int bo0 = ks0 * 8192 + trw + kh * 2048, bo1 = ks0 * 8192 + trw + (1 - kh) * 2048;
  const int bp0 = ks1 * 8192 + trw + kh * 2048, bp1 = ks1 * 8192 + trw + (1 - kh) * 2048;
  const int bv0 = ks0 * 8192 + trw + 4096, bv1 = ks1 * 8192 + trw + 4096;
  constexpr int EX = 0, ST = K1_MISC, CS = ST + 2048, PV = CS + 3072;
  const int rt = panel * 2 + mt, cb = ct * 2 + nh;
  unsigned lrow = (h2 * 32 + rr) * 16;
  unsigned vrow = (r32 >> 4) * 131072 + 8192 + kh * 512 + h2 * 256 + (lane & 15) * 16;
  GAS char* wsg = (GAS char*)ws;
  GAS char* kvb = wsg + WS_KV + (size_t)(2 * cb) * 131072 + (rt >> 3) * 16384 + (rt & 7) * 1024;
  GAS char* d0 = kh ? kvb : wsg + WS_QF + (size_t)(2 * cb) * 65536 + rt * 1024;
  GAS char* d1 = d0 + (kh ? 131072 : 65536);
  GAS char* dx = wsg + WS_XN + ((size_t)rt * 32 + 4 * ((7 + rot) & 7) + ks0) * 1024;
  float qs = kh ? 1.f : QSCALE;
  int stw = ST + arow * 32 + (nh * 2 + kh) * 8, str = ST + arow * 32;
  int cw0 = CS + ((nh * 3 + kh) * 32 + r32) * 16 + (mt * 2 + kh) * 4, cw1 = CS + ((nh * 3 + 1 - kh) * 32 + r32) * 16 + (mt * 2 + kh) * 4, cwv = CS + ((nh * 3 + 2) * 32 + r32) * 16;
  int pvw = PV + cw * 512 + r32 * 4, pvr = PV + cw * 512 + h2 * 16;
  int exw = EX + cw * 6144 + lane * 16, exr = EX + (cw ^ 4) * 6144 + lane * 16;
  PINV(lrow); PINV(vrow); PINS(kvb); PINS(d0); PINS(d1); PINS(dx); PINV(stw); PINV(str); PINV(cw0); PINV(cw1); PINV(cwv); PINV(pvw); PINV(pvr); PINV(exw); PINV(exr);
  f32x16 a0 = {}, a1 = {}, av = {};
  float sx = 0.f, sxx = 0.f, sy = 0.f, syy = 0.f, c0 = 0.f, c1 = 0.f, cv = 0.f;
  auto rd = [&](K1Frags& f, int i) LAM {
    const LAS char* sl = L + (i & 3) * K1_SLOT;
    f.a[0] = *(const LAS u32x4*)(sl + aoff0); f.b0[0] = trfrag(sl + bo0, 0, 512); f.b1[0] = trfrag(sl + bo1, 0, 512); f.bv[0] = trfrag(sl + bv0, 0, 512);
    f.a[1] = *(const LAS u32x4*)(sl + aoff1); f.b0[1] = trfrag(sl + bp0, 0, 512); f.b1[1] = trfrag(sl + bp1, 0, 512); f.bv[1] = trfrag(sl + bv1, 0, 512);
  };
#define ST2(v) do { if (ABL_ST) break; float lo_, hi_; asm volatile("v_lshlrev_b32 %0, 16, %6\n\tv_and_b32 %1, 0xffff0000, %6\n\tv_add_f32 %2, %2, %0\n\tv_add_f32 %4, %4, %1\n\tv_fmac_f32 %3, %0, %0\n\tv_fmac_f32 %5, %1, %1" \
    : "=&v"(lo_), "=&v"(hi_), "+v"(sx), "+v"(sxx), "+v"(sy), "+v"(syy) : "v"(v)); __builtin_amdgcn_sched_barrier(0); } while (0)
  auto csum = [&](const K1Frags& f, int i) LAM {
    if (!ABL_CS && ((i + kh) & 1) == mt) {
#pragma unroll
      for (int kk = 0; kk < 2; kk++)
#pragma unroll
        for (int e = 0; e < 4; e++) { c0 = dot2(f.b0[kk][e], 0x3f803f80u, c0); c1 = dot2(f.b1[kk][e], 0x3f803f80u, c1); cv = dot2(f.bv[kk][e], 0x3f803f80u, cv); }
    }
  };
#define SB() __builtin_amdgcn_sched_barrier(0)
  auto mm = [&](const K1Frags& f, K1Frags& n, int i) LAM {
    const LAS char* sl = L + ((i + 1) & 3) * K1_SLOT;
    SB(); a0 = MFMA32(f.b0[0], f.a[0], a0); SB(); n.a[0] = *(const LAS u32x4*)(sl + aoff0); n.b0[0] = trfrag(sl + bo0, 0, 512); SB(); ST2(f.a[0][0]);
    a1 = MFMA32(f.b1[0], f.a[0], a1); SB(); n.b1[0] = trfrag(sl + bo1, 0, 512); SB(); ST2(f.a[0][1]);
    av = MFMA32(f.a[0], f.bv[0], av); SB(); n.bv[0] = trfrag(sl + bv0, 0, 512); SB(); ST2(f.a[0][2]);
    a0 = MFMA32(f.b0[1], f.a[1], a0); SB(); n.a[1] = *(const LAS u32x4*)(sl + aoff1); n.b0[1] = trfrag(sl + bp0, 0, 512); SB(); ST2(f.a[0][3]);
    a1 = MFMA32(f.b1[1], f.a[1], a1); SB(); n.b1[1] = trfrag(sl + bp1, 0, 512); n.bv[1] = trfrag(sl + bv1, 0, 512); SB();
    av = MFMA32(f.a[1], f.bv[1], av); SB();
    asm volatile("" ::: "memory");
    csum(f, i);
  };
  K1Frags fa, fb;
  STAMP(1);
  raw_bar();
  STAMP(2);
  rd(fa, 0); lds_wait();
#pragma unroll 1
  for (int i = 0; i < 6; i += 2) {
    raw_bar(); mm(fa, fb, i);
    raw_bar(); mm(fb, fa, i + 1);
    STAMP(3 + (i >> 1));
  }
  raw_bar(); mm(fa, fb, 6);
  STAMP(14);
  ST2(fb.a[0][0]); ST2(fb.a[0][1]); ST2(fb.a[0][2]); ST2(fb.a[0][3]);
  csum(fb, 7);
  sx = xhalf_add(sx + sy); sxx = xhalf_add(sxx + syy); c0 = xhalf_add(c0); c1 = xhalf_add(c1); cv = xhalf_add(cv);
  { f32x2 t = {sx, sxx}; *(LAS f32x2*)(LW + stw) = t;
    *(LAS float*)(LW + cw0) = c0; *(LAS float*)(LW + cw1) = c1; *(LAS float*)(LW + cwv + (mt * 2 + kh) * 4) = cv; }
  __builtin_amdgcn_sched_barrier(0);
  a1 = MFMA32(fb.b1[0], fb.a[0], a1); av = MFMA32(fb.a[0], fb.bv[0], av); PINV(a1); PINV(av);
  __builtin_amdgcn_sched_barrier(0);
  lds_bar();
  STAMP(6);
  float mu, rs, csv;
  { const f32x4 s0 = *(const LAS f32x4*)(L + str), s1 = *(const LAS f32x4*)(L + str + 16);
    const f32x4 e0 = *(const LAS f32x4*)(L + (cw0 & ~15)), dv = *(const LAS f32x4*)(L + cwv);
    __builtin_amdgcn_sched_barrier(0);
    a1 = MFMA32(fb.b1[1], fb.a[1], a1); av = MFMA32(fb.a[1], fb.bv[1], av); PINV(a1); PINV(av);
    __builtin_amdgcn_sched_barrier(0);
    mu = ((s0[0] + s0[2]) + (s1[0] + s1[2])) * (1.f / 512.f);
    const float var = __builtin_fmaxf(((s0[1] + s0[3]) + (s1[1] + s1[3])) * (1.f / 512.f) - mu * mu, 0.f);
    rs = __builtin_amdgcn_rsqf(var + 1e-5f);
    csv = (dv[0] + dv[1]) + (dv[2] + dv[3]);
    *(LAS float*)(LW + pvw) = (e0[0] + e0[1]) + (e0[2] + e0[3]); *(LAS float*)(LW + pvw + 128) = mu; *(LAS float*)(LW + pvw + 256) = rs;
    __builtin_amdgcn_sched_barrier(0);
    a0 = MFMA32(fb.b0[0], fb.a[0], a0); a0 = MFMA32(fb.b0[1], fb.a[1], a0); PINV(a0);
    __builtin_amdgcn_sched_barrier(0);
  }
  STAMP(10);
#pragma unroll
  for (int g = 0; g < 4; g++) { f32x4 t = {a1[4 * g], a1[4 * g + 1], a1[4 * g + 2], a1[4 * g + 3]}; *(LAS f32x4*)(LW + (exw ^ 256) + g * 1024) = t; }
#pragma unroll
  for (int g = 0; g < 2; g++) { f32x4 t = {av[8 + 4 * g], av[9 + 4 * g], av[10 + 4 * g], av[11 + 4 * g]}; *(LAS f32x4*)(LW + exw + (4 + g) * 1024) = t; }
  STAMP(11);
  f32x4 cc[4], mv[2], rv[2];
#pragma unroll
  for (int g = 0; g < 4; g++) cc[g] = *(const LAS f32x4*)(L + pvr + g * 32);
#pragma unroll
  for (int g = 0; g < 2; g++) { mv[g] = *(const LAS f32x4*)(L + pvr + 128 + g * 32); rv[g] = *(const LAS f32x4*)(L + pvr + 256 + g * 32); }
  STAMP(12);
  { u32x4 xo;
#pragma unroll
    for (int e = 0; e < 4; e++) xo[e] = cvtpk((bflo(fb.a[0][e]) - mu) * rs, (bfhi(fb.a[0][e]) - mu) * rs);
    *(GAS u32x4*)(dx + lrow) = xo; }
#pragma unroll
  for (int g = 0; g < 4; g++)
#pragma unroll
    for (int e = 0; e < 4; e++) a0[4 * g + e] -= mu * cc[g][e];
#pragma unroll
  for (int g = 0; g < 2; g++)
#pragma unroll
    for (int e = 0; e < 4; e++) av[4 * g + e] -= mv[g][e] * csv;
  PINV(a0); PINV(av);
  __builtin_amdgcn_sched_barrier(0);
  STAMP(13);
  lds_bar();
  STAMP(7);
  { f32x4 t[6];
#pragma unroll
    for (int g = 0; g < 6; g++) t[g] = *(const LAS f32x4*)(L + exr + g * 1024);
    __builtin_amdgcn_sched_barrier(0);
    const float sc = rs * qs;
    u32x4 o[2], ov;
#pragma unroll
    for (int g = 0; g < 4; g++) {
      o[g >> 1][(g & 1) * 2] = cvtpk((a0[4 * g] + t[g][0]) * sc, (a0[4 * g + 1] + t[g][1]) * sc);
      o[g >> 1][(g & 1) * 2 + 1] = cvtpk((a0[4 * g + 2] + t[g][2]) * sc, (a0[4 * g + 3] + t[g][3]) * sc); }
    *(GAS u32x4*)(d0 + lrow) = o[0];
    *(GAS u32x4*)(d1 + lrow) = o[1];
#pragma unroll
    for (int g = 0; g < 2; g++) {
      ov[2 * g] = cvtpk((av[4 * g] + t[4 + g][0]) * rv[g][0], (av[4 * g + 1] + t[4 + g][1]) * rv[g][1]);
      ov[2 * g + 1] = cvtpk((av[4 * g + 2] + t[4 + g][2]) * rv[g][2], (av[4 * g + 3] + t[4 + g][3]) * rv[g][3]); }
    *(GAS u32x4*)(kvb + vrow) = ov;
  }
  STAMP(8);
  STAMP_OUT
}
constexpr int K2_LDS = 81920;
constexpr int ATT_SLOT = 16384, ATT_ONES = 4 * ATT_SLOT;
constexpr unsigned FL_GR = 0, FL_ZF = 262144, FL_UF = FL_ZF + 4096, FL_OF = FL_UF + 4096;
constexpr unsigned SPIN_MAX = 1u << 22;
typedef __amdgpu_buffer_rsrc_t rsrc_t;
DI rsrc_t mkrsrc(const void* p) { return __builtin_amdgcn_make_buffer_rsrc((void*)p, 0, 0x7fffffff, 0x00020000); }
DI u32x4 ld16(rsrc_t r, unsigned vo, unsigned so) { return __builtin_amdgcn_raw_buffer_load_b128(r, vo, so, 0); }
DI u32x4 ld16c(rsrc_t r, unsigned vo, unsigned so) { return __builtin_amdgcn_raw_buffer_load_b128(r, vo, so, 16); }
DI unsigned ld4c(rsrc_t r, unsigned vo, unsigned so) { return __builtin_amdgcn_raw_buffer_load_b32(r, vo, so, 16); }
DI void st16c(u32x4 v, rsrc_t r, unsigned vo, unsigned so) { __builtin_amdgcn_raw_buffer_store_b128(v, r, vo, so, 16); }
DI void st4c(unsigned v, rsrc_t r, unsigned vo, unsigned so) { __builtin_amdgcn_raw_buffer_store_b32(v, r, vo, so, 16); }
DI void vm_drain() { asm volatile("s_waitcnt vmcnt(0)" ::: "memory"); }
DI void poll_words(rsrc_t r, unsigned so, int lane, int n) {
  const unsigned vo = (lane < n ? lane : 0) * 4;
  for (unsigned it = 0; it < SPIN_MAX; it++) {
    const unsigned v = ld4c(r, vo, so);
    if (WALL(v != 0u)) break;
    __builtin_amdgcn_s_sleep(2);
  }
  asm volatile("" ::: "memory");
}
DI float gelu_t(float x) {
  const float a = x * __builtin_fmaf(x * x, -0.10294324f, -2.3022082f);
  return x * __builtin_amdgcn_rcpf(1.f + __builtin_amdgcn_exp2f(a));
}

DI void attn_body(char* ws, int b, char* lds) {
  const int tid = TIDX(), lane = tid & 63; const int wid = __builtin_amdgcn_readfirstlane(tid >> 6);
  const int xcd = b & 7, j = b >> 3, h = xcd * 4 + (j >> 3), qt = (j & 7) * 8 + wid;
  GSTAMP(0);
  const unsigned lds0 = (unsigned)(uintptr_t)lds;
  const char* gb = ws + WS_KV + (size_t)h * 131072;
  unsigned goff = wid * 2048 + lane * 16;
  const unsigned mb = lds0 + wid * 2048;
  u32x4 qf;
  { const char* qp = ws + WS_QF + ((size_t)h * 64 + qt) * 1024; const unsigned lo = lane * 16;
    asm volatile("global_load_dwordx4 %0, %1, %2" : "=v"(qf) : "v"(lo), "s"(qp) : "memory"); }
  glds2(gb, goff, mb); glds2(gb, goff + 16384, mb + ATT_SLOT);
  goff += 32768;
  { const unsigned ov = (tid & 11) == 0 ? 0x3f803f80u : 0u; u32x4 ones = {ov, ov, ov, ov};
    *(LAS u32x4*)((LAS char*)lds + ATT_ONES + tid * 16) = ones; }
  const bool isd = (lane & 16) == 0;
  const unsigned kl = lds0 + lane * 16;
  const unsigned vb = lds0 + (isd ? 8192 + (lane >> 5) * 256 + (lane & 15) * 16 : ATT_ONES + (lane & 15) * 16);
  const unsigned dm = isd ? 0xffffffffu : 0u;
  asm volatile("s_waitcnt vmcnt(2) lgkmcnt(0)\n\ts_barrier" : "+v"(qf) :: "memory");
  f32x16 negm, o = {};
  {
    const LAS char* kp = (const LAS char*)lds + lane * 16;
    const f32x16 z = {};
    const f32x16 s0 = MFMA32(*(const LAS u32x4*)kp, qf, z), s1 = MFMA32(*(const LAS u32x4*)(kp + 1024), qf, z);
    float m = __builtin_fmaxf(s0[0], s1[0]);
#pragma unroll
    for (int r = 1; r < 16; r++) m = __builtin_fmaxf(m, __builtin_fmaxf(s0[r], s1[r]));
    auto rr = __builtin_amdgcn_permlane32_swap(F2U(m), F2U(m), false, false);
    m = -(__builtin_fmaxf(U2F(rr[0]), U2F(rr[1])) + 64.f);
#pragma unroll
    for (int r = 0; r < 16; r++) negm[r] = m;
  }
  GSTAMP(1);
  { unsigned c, sc, t0, sm0;
    asm volatile(ATTN_LOOP_ASM
      : [o] "+v"(o), [goff] "+v"(goff), [c] "=&s"(c), [sc] "=&s"(sc), [t0] "=&s"(t0), [sm0] "=&s"(sm0)
      : [q] "v"(qf), [negm] "v"(negm), [kl] "v"(kl), [vb] "v"(vb), [dm] "v"(dm), [gb] "s"(gb), [mb] "s"(mb)
      : "memory", "scc", ATTN_CLOBBERS); }
  GSTAMP(2);
  {
    float chk = o[8];
#pragma unroll
    for (int r = 0; r < 8; r++) chk = __builtin_fmaxf(chk, __builtin_fabsf(o[r]));
    const bool bad = !(o[8] <= 1.2676506e30f) || !(chk <= 3.0e38f) || ATT_FORCE_FALLBACK;
    if (__builtin_expect(WANY(bad), 0)) {
      const rsrc_t kv = mkrsrc(gb);
      const f32x16 z = {};
      float m = -3.0e38f;
#pragma unroll 1
      for (int t = 0; t < 64; t++) {
        const f32x16 s = MFMA32(ld16(kv, lane * 16, (t >> 3) * 16384 + (t & 7) * 1024), qf, z);
#pragma unroll
        for (int r = 0; r < 16; r++) m = __builtin_fmaxf(m, s[r]);
      }
      auto rr = __builtin_amdgcn_permlane32_swap(F2U(m), F2U(m), false, false);
      m = -__builtin_fmaxf(U2F(rr[0]), U2F(rr[1]));
#pragma unroll
      for (int r = 0; r < 16; r++) { negm[r] = m; o[r] = 0.f; }
      const unsigned pad = (lane & 11) == 0 ? 0x3f803f80u : 0u;
#pragma unroll 1
      for (int t = 0; t < 64; t++) {
        const unsigned so = (t >> 3) * 16384 + (t & 7) * 1024;
        const f32x16 s = MFMA32(ld16(kv, lane * 16, so), qf, negm);
#pragma unroll
        for (int u = 0; u < 2; u++) {
          u32x4 v = ld16(kv, 8192 + u * 512 + (lane >> 5) * 256 + (lane & 15) * 16, so), p;
#pragma unroll
          for (int r = 0; r < 4; r++) { v[r] = isd ? v[r] : pad; p[r] = cvtpk(__builtin_amdgcn_exp2f(s[8 * u + 2 * r]), __builtin_amdgcn_exp2f(s[8 * u + 2 * r + 1])); }
          o = MFMA32(v, p, o);
        }
      }
    }
  }
  const float rl = __builtin_amdgcn_rcpf(o[8]);
  u32x4 ov;
#pragma unroll
  for (int r = 0; r < 4; r++) ov[r] = cvtpk(o[2 * r] * rl, o[2 * r + 1] * rl);
  st16c(ov, mkrsrc(ws + WS_AF), lane * 16, (qt * 88 + h) * 1024);
  vm_drain();
  if (lane == 0) st4c(1u, mkrsrc(ws + WS_FL), 0, FL_OF + (qt >> 1) * 256 + (h * 2 + (qt & 1)) * 4);
  GSTAMP(3);
}

DI void gemm_body(const char* x, const char* w_sp, const char* b_sp, char* out, char* ws, int gb, char* lds) {
  const int tid = TIDX(), lane = tid & 63; const int wid = __builtin_amdgcn_readfirstlane(tid >> 6);
  const int xcd = gb & 7, j = gb >> 3, p = xcd * 4 + (j >> 3), nb = j & 7;
  const int r32 = lane & 31, h2 = lane >> 5;
  const unsigned lds0 = (unsigned)(uintptr_t)lds, l16 = lane * 16;
  const LAS char* L = (const LAS char*)lds;
  LAS char* LW = (LAS char*)lds;
  const rsrc_t fl = mkrsrc(ws + WS_FL), af = mkrsrc(ws + WS_AF);
  __builtin_amdgcn_s_setprio(3);
  GSTAMP(0);
  auto fill64 = [&](const char* src) LAM {
    const unsigned mb = lds0 + wid * 8192 + 4096;
    asm volatile("s_mov_b32 m0, %2\n\ts_nop 0\n\t"
                 "global_load_lds_dwordx4 %0, %1 offset:-4096\n\tglobal_load_lds_dwordx4 %0, %1 offset:-3072\n\tglobal_load_lds_dwordx4 %0, %1 offset:-2048\n\tglobal_load_lds_dwordx4 %0, %1 offset:-1024\n\t"
                 "global_load_lds_dwordx4 %0, %1\n\tglobal_load_lds_dwordx4 %0, %1 offset:1024\n\tglobal_load_lds_dwordx4 %0, %1 offset:2048\n\tglobal_load_lds_dwordx4 %0, %1 offset:3072"
                 :: "v"(l16), "s"(src + 4096), "s"(mb) : "memory");
  };
  if (nb < 7) {
    const int nq = wid & 3, kh = wid >> 2;
    unsigned zp[16];
    constexpr int SCR = 65536, STP = SCR + 8192, STF = STP + 2048;
    { fill64(ws + WS_XN + (size_t)p * 65536 + wid * 8192);
      const rsrc_t wr = mkrsrc(ws + WS_WIN + (size_t)(kh * 28 + nb * 4 + nq) * 32768);
      u32x4 bw[8];
#pragma unroll
      for (int k = 0; k < 8; k++) bw[k] = ld16(wr, l16, k * 1024);
      wait_bar<8>();
      f32x16 z0 = {}, z1 = {};
#pragma unroll 1
      for (int i = 0; i < 4; i++) {
        const unsigned nx = ((i + 1) & 3) * 8192;
#pragma unroll
        for (int k = 0; k < 8; k++) {
          const u32x4 xa = *(const LAS u32x4*)(L + l16 + i * 8192 + k * 1024), xb = *(const LAS u32x4*)(L + l16 + 32768 + i * 8192 + k * 1024);
          z0 = MFMA32(bw[k], xa, z0); z1 = MFMA32(bw[k], xb, z1);
          if (i < 3) bw[k] = ld16(wr, l16, nx + k * 1024);
        }
      }
      GSTAMP(1);
      float s0 = 0.f, q0 = 0.f, s1 = 0.f, q1 = 0.f;
#pragma unroll
      for (int r = 0; r < 16; r++) { z0[r] = gelu_t(z0[r]); z1[r] = gelu_t(z1[r]); }
#pragma unroll
      for (int r = 0; r < 8; r++) { zp[r] = cvtpk(z0[2 * r], z0[2 * r + 1]); zp[8 + r] = cvtpk(z1[2 * r], z1[2 * r + 1]); }
      if (kh) {
#pragma unroll
        for (int r = 0; r < 16; r++) { s0 += z0[r]; q0 = __builtin_fmaf(z0[r], z0[r], q0); s1 += z1[r]; q1 = __builtin_fmaf(z1[r], z1[r], q1); }
        s0 = xhalf_add(s0); q0 = xhalf_add(q0); s1 = xhalf_add(s1); q1 = xhalf_add(q1);
        f32x2 t = h2 ? (f32x2){s1, q1} : (f32x2){s0, q0};
        *(LAS f32x2*)(LW + STP + nq * 512 + lane * 8) = t;
      }
    }
    lds_bar();
    GSTAMP(2);
    const int rot = 2 * nb + 5 * (p & 3);
    const char* wsp = w_sp + (size_t)p * 64 * 4096;
    unsigned av0, av1;
    { const int row = wid * 8 + (lane >> 4); av0 = row * 4096 + (((lane & 15) ^ (row & 15)) << 4); av1 = (row + 4) * 4096 + (((lane & 15) ^ ((row + 4) & 15)) << 4) - 1024; }
    auto astage = [&](int i) LAM {
      const unsigned mb = lds0 + (i & 3) * 16384 + wid * 2048;
      asm volatile("s_mov_b32 m0, %3\n\ts_nop 0\n\tglobal_load_lds_dwordx4 %0, %2\n\tglobal_load_lds_dwordx4 %1, %2 offset:1024"
                   :: "v"(av0), "v"(av1), "s"(wsp + ((i + rot) & 15) * 256), "s"(mb) : "memory");
    };
    if (wid != 4) { astage(0); astage(1); astage(2); }
    if (wid == 4) {
      float s = 0.f, q = 0.f;
#pragma unroll
      for (int w = 0; w < 4; w++) { const f32x2 t = *(const LAS f32x2*)(L + STP + w * 512 + lane * 8); s += t[0]; q += t[1]; }
      const u32x4 g = {F2U(s), 1u, F2U(q), 1u};
      st16c(g, fl, l16, FL_GR + (p * 8 + nb) * 1024);
      for (unsigned it = 0; it < SPIN_MAX; it++) {
        bool ok = true; s = 0.f; q = 0.f;
#pragma unroll
        for (int w = 0; w < 7; w++) { const u32x4 t = ld16c(fl, l16, FL_GR + (p * 8 + w) * 1024); ok = ok && t[1] == 1u && t[3] == 1u; s += U2F(t[0]); q += U2F(t[2]); }
        if (WALL(ok)) break;
        __builtin_amdgcn_s_sleep(2);
      }
      const float mu = s * (1.f / 896.f);
      const float rs = __builtin_amdgcn_rsqf(__builtin_fmaxf(q * (1.f / 896.f) - mu * mu, 0.f) + 1e-5f);
      const f32x2 t = {rs, -mu * rs};
      *(LAS f32x2*)(LW + STF + lane * 8) = t;
      astage(0); astage(1); astage(2);
    }
    lds_bar();
    GSTAMP(3);
    const rsrc_t zr = mkrsrc(ws + WS_Z2 + (size_t)(nb * 4 + nq) * 131072);
    if (kh) {
      LAS char* tw = LW + SCR + nq * 2048 + r32 * 64 + h2 * 8;
      const int q4 = (lane & 15) >> 2, pp = lane & 3, ch = ((lane >> 4) & 1) * 2 + (pp >> 1);
#pragma unroll
      for (int T = 0; T < 2; T++) {
        const f32x2 st = *(const LAS f32x2*)(L + STF + (T * 32 + r32) * 8);
#pragma unroll
        for (int g = 0; g < 4; g++) {
          const unsigned a = zp[T * 8 + 2 * g], c = zp[T * 8 + 2 * g + 1];
          const u32x2 v = {cvtpk(__builtin_fmaf(bflo(a), st[0], st[1]), __builtin_fmaf(bfhi(a), st[0], st[1])), cvtpk(__builtin_fmaf(bflo(c), st[0], st[1]), __builtin_fmaf(bfhi(c), st[0], st[1]))};
          *(LAS u32x2*)(tw + ((g ^ ((r32 >> 1) & 3)) << 4)) = v;
        }
#pragma unroll
        for (int ks = 0; ks < 2; ks++) {
          const int s0 = 16 * ks + 8 * h2 + q4, s1 = s0 + 4;
          const u32x2 lo = trread(L + SCR + nq * 2048 + s0 * 64 + ((ch ^ ((s0 >> 1) & 3)) << 4) + (pp & 1) * 8);
          const u32x2 hi = trread(L + SCR + nq * 2048 + s1 * 64 + ((ch ^ ((s1 >> 1) & 3)) << 4) + (pp & 1) * 8);
          const u32x4 f = {lo[0], lo[1], hi[0], hi[1]};
          st16c(f, zr, l16, (p * 4 + T * 2 + ks) * 1024);
        }
      }
      vm_drain();
    }
    raw_bar();
    if (wid == 4) {
      if (lane == 0) st4c(1u, fl, 0, FL_ZF + nb * 128 + p * 4);
      poll_words(fl, FL_ZF + nb * 128, lane, 32);
    }
    raw_bar();
    GSTAMP(4);
    f32x16 g0 = {}, g1 = {};
    { u32x4 bz[8];
      const unsigned zo = kh * 4096;
#pragma unroll
      for (int k = 0; k < 8; k++) bz[k] = ld16c(zr, l16 + (k & 3) * 1024, (((k >> 2) + rot) & 15) * 8192 + zo);
      int ao[4];
#pragma unroll
      for (int k = 0; k < 4; k++) ao[k] = r32 * 256 + (((8 * kh + 2 * k + h2) ^ (r32 & 15)) << 4);
      auto step = [&](int i, int par, bool dma, bool pre) LAM {
        if (dma) astage(i + 3);
        const LAS char* sl = L + (i & 3) * 16384;
        const unsigned so = ((i + 2 + rot) & 15) * 8192 + zo;
#pragma unroll
        for (int k = 0; k < 4; k++) {
          const u32x4 wa = *(const LAS u32x4*)(sl + ao[k]), wb = *(const LAS u32x4*)(sl + ao[k] + 8192);
          g0 = MFMA32(bz[par * 4 + k], wa, g0); g1 = MFMA32(bz[par * 4 + k], wb, g1);
          if (pre) bz[par * 4 + k] = ld16c(zr, l16 + k * 1024, so);
        }
      };
      wait_bar<8>();
      step(0, 0, true, true); lds_bar(); step(1, 1, true, true); lds_bar();
#pragma unroll 1
      for (int i = 2; i < 12; i += 2) {
        step(i, 0, true, true); wait_bar<16>(); step(i + 1, 1, true, true); wait_bar<16>();
      }
      step(12, 0, true, true); wait_bar<16>(); step(13, 1, false, true); wait_bar<14>();
      step(14, 0, false, false); wait_bar<8>(); step(15, 1, false, false);
    }
    GSTAMP(5);
    lds_bar();
    if (kh) {
#pragma unroll
      for (int g = 0; g < 4; g++) {
        const f32x4 a = {g0[4 * g], g0[4 * g + 1], g0[4 * g + 2], g0[4 * g + 3]}, c = {g1[4 * g], g1[4 * g + 1], g1[4 * g + 2], g1[4 * g + 3]};
        *(LAS f32x4*)(LW + nq * 8192 + g * 1024 + l16) = a; *(LAS f32x4*)(LW + nq * 8192 + 4096 + g * 1024 + l16) = c;
      }
    }
    const float b0 = U2F((unsigned)*(const unsigned short*)(b_sp + (p * 64 + r32) * 2) << 16);
    const float b1 = U2F((unsigned)*(const unsigned short*)(b_sp + (p * 64 + 32 + r32) * 2) << 16);
    lds_bar();
    if (!kh) {
#pragma unroll
      for (int T = 0; T < 2; T++) {
        const f32x16& gg = T ? g1 : g0; const float bb = T ? b1 : b0;
#pragma unroll
        for (int s = 0; s < 2; s++) {
          u32x4 u;
#pragma unroll
          for (int g = 0; g < 2; g++) {
            const int o = 8 * s + 4 * g;
            const f32x4 t = *(const LAS f32x4*)(L + nq * 8192 + T * 4096 + (2 * s + g) * 1024 + l16);
            const unsigned a = zp[T * 8 + 4 * s + 2 * g], c = zp[T * 8 + 4 * s + 2 * g + 1];
            u[2 * g] = cvtpk((gg[o] + t[0] + bb) * bflo(a), (gg[o + 1] + t[1] + bb) * bfhi(a));
            u[2 * g + 1] = cvtpk((gg[o + 2] + t[2] + bb) * bflo(c), (gg[o + 3] + t[3] + bb) * bfhi(c));
          }
          st16c(u, af, l16, ((p * 2 + T) * 88 + 32 + nb * 8 + nq * 2 + s) * 1024);
        }
      }
      vm_drain();
    }
    lds_bar();
    if (wid == 0 && lane == 0) st4c(1u, fl, 0, FL_UF + p * 128 + nb * 4);
    GSTAMP(6);
  }
  { const int mt = wid & 1, nt = (wid >> 1) & 1, kh = wid >> 2;
    fill64(ws + WS_WC + (size_t)(nb * 2 + (wid >> 2)) * 90112 + (wid & 3) * 8192);
    const rsrc_t wc = mkrsrc(ws + WS_WC + (size_t)(nb * 2 + nt) * 90112);
    const unsigned arow = (p * 2 + mt) * 90112;
    f32x16 acc = {};
    u32x4 fa[16];
    { u32x4 fw[7];
      const unsigned k0 = (32 + kh * 28) * 1024;
#pragma unroll
      for (int k = 0; k < 7; k++) fw[k] = ld16(wc, l16, k0 + k * 1024);
      if (wid == 0) poll_words(fl, FL_UF + p * 128, lane, 7);
      raw_bar();
      GSTAMP(7);
#pragma unroll
      for (int k = 0; k < 7; k++) fa[k] = ld16c(af, l16, arow + k0 + k * 1024);
#pragma unroll 1
      for (int i = 0; i < 4; i++) {
        const unsigned nx = k0 + (i + 1) * 7168;
#pragma unroll
        for (int k = 0; k < 7; k++) {
          acc = MFMA32(fw[k], fa[k], acc);
          if (i < 3) { fw[k] = ld16(wc, l16, nx + k * 1024); fa[k] = ld16c(af, l16, arow + nx + k * 1024); }
        }
      }
    }
    const unsigned orow = (p * 64 + mt * 32 + r32) * 1024 + (nb * 64 + nt * 32 + kh * 16 + h2 * 4) * 2;
    const u32x2 x0 = *(const u32x2*)(x + orow), x1 = *(const u32x2*)(x + orow + 16);
    GSTAMP(8);
    vm_drain();
    if (wid == 0) poll_words(fl, FL_OF + p * 256, lane, 64);
    raw_bar();
    GSTAMP(9);
#pragma unroll
    for (int k = 0; k < 16; k++) fa[k] = ld16c(af, l16 + (k & 3) * 1024, arow + kh * 16384 + (k >> 2) * 4096);
#pragma unroll
    for (int k = 0; k < 16; k++) acc = MFMA32(*(const LAS u32x4*)(L + nt * 32768 + kh * 16384 + k * 1024 + l16), fa[k], acc);
    { LAS char* ex = LW + 65536 + wid * 2048 + l16;
      f32x4 a, c;
#pragma unroll
      for (int e = 0; e < 4; e++) { a[e] = kh ? acc[e] : acc[8 + e]; c[e] = kh ? acc[4 + e] : acc[12 + e]; }
      *(LAS f32x4*)ex = a; *(LAS f32x4*)(ex + 1024) = c;
      lds_bar();
      const LAS char* er = L + 65536 + (wid ^ 4) * 2048 + l16;
      a = *(const LAS f32x4*)er; c = *(const LAS f32x4*)(er + 1024);
#pragma unroll
      for (int e = 0; e < 4; e++) { a[e] += kh ? acc[8 + e] : acc[e]; c[e] += kh ? acc[12 + e] : acc[4 + e]; }
      const u32x2 y0 = {cvtpk(a[0] + bflo(x0[0]), a[1] + bfhi(x0[0])), cvtpk(a[2] + bflo(x0[1]), a[3] + bfhi(x0[1]))};
      const u32x2 y1 = {cvtpk(c[0] + bflo(x1[0]), c[1] + bfhi(x1[0])), cvtpk(c[2] + bflo(x1[1]), c[3] + bfhi(x1[1]))};
      *(u32x2*)(out + orow) = y0; *(u32x2*)(out + orow + 16) = y1;
    }
    GSTAMP(10);
  }
}

NOPK __global__ __launch_bounds__(512, 4) void k2_kernel(const char* x, const char* w_sp, const char* b_sp, char* out, char* ws) {
  extern __shared__ __attribute__((aligned(16))) char lds[];
  if (BIDX() < 256) attn_body(ws, BIDX(), lds);
  else gemm_body(x, w_sp, b_sp, out, ws, BIDX() - 256, lds);
}
extern "C" void kernel_launch(void* const* d_in, const int* in_sizes, int n_in, void* d_out, int out_size, void* d_ws, size_t ws_size, hipStream_t stream) {
  (void)in_sizes; (void)n_in; (void)out_size; (void)ws_size;
  (void)hipFuncSetAttribute((const void*)k1_kernel, hipFuncAttributeMaxDynamicSharedMemorySize, K1_LDS);
  (void)hipFuncSetAttribute((const void*)k2_kernel, hipFuncAttributeMaxDynamicSharedMemorySize, K2_LDS);
  k1_kernel<<<256, 768, K1_LDS, stream>>>((const char*)d_in[0], (const char*)d_in[1], (const char*)d_in[2], (const char*)d_in[4], (const char*)d_in[7], (char*)d_ws);
  k2_kernel<<<512, 512, K2_LDS, stream>>>((const char*)d_in[0], (const char*)d_in[5], (const char*)d_in[6], (char*)d_out, (char*)d_ws);
}
```

```cpp
#include <hip/hip_runtime.h>
#include <hip/hip_bf16.h>
#include <stdint.h>

typedef __attribute__((ext_vector_type(16))) float f32x16;
typedef __attribute__((ext_vector_type(4))) float f32x4;
typedef __attribute__((ext_vector_type(2))) float f32x2;
typedef __attribute__((ext_vector_type(4))) unsigned u32x4;
typedef __attribute__((ext_vector_type(2))) unsigned u32x2;
typedef __attribute__((ext_vector_type(2))) __bf16 bf2;
typedef __attribute__((ext_vector_type(8))) __bf16 bfv8;
typedef __attribute__((ext_vector_type(4))) short s16x4;
#define LAS __attribute__((address_space(3)))
#if defined(__HIP_DEVICE_COMPILE__)
#define NOPK __attribute__((target("no-packed-fp32-ops")))
#else
#define NOPK
#endif
#define DI NOPK __device__ __forceinline__
#define LAM NOPK __attribute__((always_inline))
#define U2F(x) __builtin_bit_cast(float, (unsigned)(x))
#define F2U(x) __builtin_bit_cast(unsigned, (float)(x))
#define WANY(p) (__builtin_amdgcn_ballot_w64(p) != 0ull)
#define SOFF(x) ((unsigned)__builtin_amdgcn_readfirstlane((int)(x)))
#define TIDX() ((int)__builtin_amdgcn_workitem_id_x())
#define BIDX() ((int)__builtin_amdgcn_workgroup_id_x())
#define ATT_OFS 88.f
#define PF_DLY 3
#define GPRIO_Z 3
#define GPRIO_V 3
#define MFMA32(a, b, c) __builtin_amdgcn_mfma_f32_32x32x16_bf16(__builtin_bit_cast(bfv8, a), __builtin_bit_cast(bfv8, b), c, 0, 0, 0)

constexpr size_t WS_QF = 0;
constexpr size_t WS_KV = WS_QF + (2u << 20);
constexpr size_t WS_XN = WS_KV + (4u << 20);
constexpr size_t WS_WIN = WS_XN + (2u << 20);
constexpr size_t WS_WC = WS_WIN + 896u * 1024;
constexpr size_t WS_Z2 = WS_WC + 1408u * 1024;
constexpr size_t WS_AF = WS_Z2 + 3584u * 1024;
constexpr size_t WS_ZR = WS_AF + 5632u * 1024;
constexpr size_t WS_ZS = WS_ZR + 3584u * 1024;
constexpr size_t WS_FL = WS_ZS + 256u * 1024;
constexpr size_t FL_BYTES = 32u * 1024;

DI unsigned cvtpk(float lo, float hi) { f32x2 t = {lo, hi}; return __builtin_bit_cast(unsigned, __builtin_convertvector(t, bf2)); }
DI float bflo(unsigned v) { return U2F(v << 16); }
DI float bfhi(unsigned v) { return U2F(v & 0xffff0000u); }
DI float xhalf_add(float v) {
  auto r = __builtin_amdgcn_permlane32_swap(F2U(v), F2U(v), false, false);
  return U2F(r[0]) + U2F(r[1]);
}
DI void glds2(const void* sbase, unsigned voff, unsigned ldsb) {
  unsigned sv;
  asm volatile("s_mov_b32 %0, m0\n\ts_mov_b32 m0, %3\n\ts_nop 0\n\tglobal_load_lds_dwordx4 %1, %2\n\tglobal_load_lds_dwordx4 %1, %2 offset:1024\n\ts_mov_b32 m0, %0"
               : "=&s"(sv) : "v"(voff), "s"(sbase), "s"(ldsb) : "memory");
}
template <int N> DI void wait_bar() { asm volatile("s_waitcnt vmcnt(%0) lgkmcnt(0)\n\ts_barrier" ::"i"(N) : "memory"); }
DI void lds_wait() { asm volatile("" ::: "memory"); __builtin_amdgcn_s_waitcnt(0xC07F); asm volatile("" ::: "memory"); }
DI void raw_bar() { asm volatile("s_barrier" ::: "memory"); }
DI void lds_bar() { lds_wait(); raw_bar(); }
DI u32x2 trread(const LAS char* p) { return __builtin_bit_cast(u32x2, __builtin_amdgcn_ds_read_tr16_b64_v4i16((LAS s16x4*)p)); }
DI u32x4 trfrag(const LAS char* p, int o0, int o1) { u32x2 a = trread(p + o0), b = trread(p + o1); u32x4 r = {a[0], a[1], b[0], b[1]}; return r; }
#define PINV(x) asm volatile("" : "+v"(x))
DI float gelu_t(float x) {
  const float a = x * __builtin_fmaf(x * x, -0.10294324f, -2.3022082f);
  return x * __builtin_amdgcn_rcpf(1.f + __builtin_amdgcn_exp2f(a));
}
DI int lane_id() { int l; asm volatile("v_mbcnt_lo_u32_b32 %0, -1, 0\n\tv_mbcnt_hi_u32_b32 %0, -1, %0" : "=v"(l)); return l; }
#define ATTN_LOOP_ASM \
  "s_mov_b32 %[sm0], m0\n\t" \
  "ds_read_b128 v[104:107], %[kl]\n\t" \
  "ds_read_b128 v[108:111], %[vb]\n\t" \
  "ds_read_b128 v[112:115], %[vb] offset:512\n\t" \
  "v_mov_b32 v100, 0\n\t" \
  "v_mov_b32 v101, 0\n\t" \
  "v_mov_b32 v102, 0\n\t" \
  "v_mov_b32 v103, 0\n\t" \
  "v_mov_b32 v88, 0\n\t" \
  "v_mov_b32 v89, 0\n\t" \
  "v_mov_b32 v90, 0\n\t" \
  "v_mov_b32 v91, 0\n\t" \
  "v_mov_b32 v92, 0\n\t" \
  "v_mov_b32 v93, 0\n\t" \
  "v_mov_b32 v94, 0\n\t" \
  "v_mov_b32 v95, 0\n\t" \
  "v_mov_b32 v120, 0\n\t" \
  "v_mov_b32 v121, 0\n\t" \
  "v_mov_b32 v122, 0\n\t" \
  "v_mov_b32 v123, 0\n\t" \
  "v_mov_b32 v124, %[kl]\n\t" \
  "v_mov_b32 v126, %[vb]\n\t" \
  "s_mov_b32 %[c], 0\n\t" \
  "s_mov_b32 %[sc], 0\n\t" \
  "s_waitcnt lgkmcnt(2)\n\t" \
  "v_mfma_f32_32x32x16_bf16 v[64:79], v[104:107], %[q], %[negm]\n\t" \
  "ds_read_b128 v[104:107], %[kl] offset:1024\n\t" \
  "s_waitcnt lgkmcnt(0)\n\t" \
  "s_nop 10\n\t" \
  ".Lloop%=:\n\t" \
  "s_waitcnt lgkmcnt(1)\n\t" \
  "v_exp_f32 v64, v64\n\t" \
  "v_exp_f32 v65, v65\n\t" \
  "v_cvt_pk_bf16_f32 v103, v94, v95\n\t" \
  "v_mfma_f32_32x32x16_bf16 v[80:95], v[104:107], %[q], %[negm]\n\t" \
  "ds_read_b128 v[104:107], v124 offset:2048\n\t" \
  "v_exp_f32 v66, v66\n\t" \
  "ds_read_b128 v[116:119], v126 offset:1024\n\t" \
  "v_exp_f32 v67, v67\n\t" \
  "s_setprio 2\n\t" \
  "v_cvt_pk_bf16_f32 v96, v64, v65\n\t" \
  "v_exp_f32 v68, v68\n\t" \
  "v_exp_f32 v69, v69\n\t" \
  "s_xor_b32 %[t0], %[sc], 0x8000\n\t" \
  "v_cvt_pk_bf16_f32 v97, v66, v67\n\t" \
  "v_mfma_f32_32x32x16_bf16 %[o], v[120:123], v[100:103], %[o]\n\t" \
  "ds_read_b128 v[120:123], v126 offset:1536\n\t" \
  "v_exp_f32 v70, v70\n\t" \
  "v_exp_f32 v71, v71\n\t" \
  "s_add_u32 m0, %[t0], %[mb]\n\t" \
  "v_cvt_pk_bf16_f32 v98, v68, v69\n\t" \
  "v_exp_f32 v72, v72\n\t" \
  "v_exp_f32 v73, v73\n\t" \
  "v_cvt_pk_bf16_f32 v99, v70, v71\n\t" \
  "v_exp_f32 v74, v74\n\t" \
  "v_exp_f32 v75, v75\n\t" \
  "v_cvt_pk_bf16_f32 v100, v72, v73\n\t" \
  "v_mfma_f32_32x32x16_bf16 %[o], v[108:111], v[96:99], %[o]\n\t" \
  "v_exp_f32 v76, v76\n\t" \
  "v_exp_f32 v77, v77\n\t" \
  "v_cvt_pk_bf16_f32 v101, v74, v75\n\t" \
  "v_exp_f32 v78, v78\n\t" \
  "v_exp_f32 v79, v79\n\t" \
  "v_cvt_pk_bf16_f32 v102, v76, v77\n\t" \
  "s_waitcnt lgkmcnt(1)\n\t" \
  "v_exp_f32 v80, v80\n\t" \
  "v_exp_f32 v81, v81\n\t" \
  "v_cvt_pk_bf16_f32 v103, v78, v79\n\t" \
  "v_mfma_f32_32x32x16_bf16 v[64:79], v[104:107], %[q], %[negm]\n\t" \
  "ds_read_b128 v[104:107], v124 offset:3072\n\t" \
  "v_exp_f32 v82, v82\n\t" \
  "ds_read_b128 v[108:111], v126 offset:2048\n\t" \
  "v_exp_f32 v83, v83\n\t" \
  "v_cvt_pk_bf16_f32 v96, v80, v81\n\t" \
  "v_exp_f32 v84, v84\n\t" \
  "v_exp_f32 v85, v85\n\t" \
  "v_cvt_pk_bf16_f32 v97, v82, v83\n\t" \
  "v_mfma_f32_32x32x16_bf16 %[o], v[112:115], v[100:103], %[o]\n\t" \
  "ds_read_b128 v[112:115], v126 offset:2560\n\t" \
  "v_exp_f32 v86, v86\n\t" \
  "v_exp_f32 v87, v87\n\t" \
  "v_cvt_pk_bf16_f32 v98, v84, v85\n\t" \
  "v_exp_f32 v88, v88\n\t" \
  "s_setprio 3\n\t" \
  "v_exp_f32 v89, v89\n\t" \
  "global_load_lds_dwordx4 %[goff], %[gb]\n\t" \
  "v_cvt_pk_bf16_f32 v99, v86, v87\n\t" \
  "v_exp_f32 v90, v90\n\t" \
  "s_setprio 2\n\t" \
  "v_exp_f32 v91, v91\n\t" \
  "v_cvt_pk_bf16_f32 v100, v88, v89\n\t" \
  "v_mfma_f32_32x32x16_bf16 %[o], v[116:119], v[96:99], %[o]\n\t" \
  "v_exp_f32 v92, v92\n\t" \
  "v_exp_f32 v93, v93\n\t" \
  "v_cvt_pk_bf16_f32 v101, v90, v91\n\t" \
  "v_exp_f32 v94, v94\n\t" \
  "v_exp_f32 v95, v95\n\t" \
  "v_cvt_pk_bf16_f32 v102, v92, v93\n\t" \
  "s_waitcnt lgkmcnt(1)\n\t" \
  "v_exp_f32 v64, v64\n\t" \
  "v_exp_f32 v65, v65\n\t" \
  "v_cvt_pk_bf16_f32 v103, v94, v95\n\t" \
  "v_mfma_f32_32x32x16_bf16 v[80:95], v[104:107], %[q], %[negm]\n\t" \
  "ds_read_b128 v[104:107], v124 offset:4096\n\t" \
  "v_exp_f32 v66, v66\n\t" \
  "ds_read_b128 v[116:119], v126 offset:3072\n\t" \
  "v_exp_f32 v67, v67\n\t" \
  "s_setprio 1\n\t" \
  "v_cvt_pk_bf16_f32 v96, v64, v65\n\t" \
  "v_exp_f32 v68, v68\n\t" \
  "v_exp_f32 v69, v69\n\t" \
  "s_cmp_lg_u32 %[c], 3\n\t" \
  "v_cvt_pk_bf16_f32 v97, v66, v67\n\t" \
  "v_mfma_f32_32x32x16_bf16 %[o], v[120:123], v[100:103], %[o]\n\t" \
  "ds_read_b128 v[120:123], v126 offset:3584\n\t" \
  "v_exp_f32 v70, v70\n\t" \
  "v_exp_f32 v71, v71\n\t" \
  "s_cbranch_scc1 .Lnf%=\n\t" \
  "global_store_dwordx4 %[l16], %[pre], %[op] sc1\n\t" \
  ".Lnf%=:\n\t" \
  "v_cvt_pk_bf16_f32 v98, v68, v69\n\t" \
  "v_exp_f32 v72, v72\n\t" \
  "v_exp_f32 v73, v73\n\t" \
  "v_cvt_pk_bf16_f32 v99, v70, v71\n\t" \
  "v_exp_f32 v74, v74\n\t" \
  "v_exp_f32 v75, v75\n\t" \
  "v_cvt_pk_bf16_f32 v100, v72, v73\n\t" \
  "v_mfma_f32_32x32x16_bf16 %[o], v[108:111], v[96:99], %[o]\n\t" \
  "v_exp_f32 v76, v76\n\t" \
  "v_exp_f32 v77, v77\n\t" \
  "v_cvt_pk_bf16_f32 v101, v74, v75\n\t" \
  "v_exp_f32 v78, v78\n\t" \
  "v_exp_f32 v79, v79\n\t" \
  "v_cvt_pk_bf16_f32 v102, v76, v77\n\t" \
  "s_waitcnt lgkmcnt(1)\n\t" \
  "v_exp_f32 v80, v80\n\t" \
  "v_exp_f32 v81, v81\n\t" \
  "v_cvt_pk_bf16_f32 v103, v78, v79\n\t" \
  "v_mfma_f32_32x32x16_bf16 v[64:79], v[104:107], %[q], %[negm]\n\t" \
  "ds_read_b128 v[104:107], v124 offset:5120\n\t" \
  "v_exp_f32 v82, v82\n\t" \
  "ds_read_b128 v[108:111], v126 offset:4096\n\t" \
  "v_exp_f32 v83, v83\n\t" \
  "v_cvt_pk_bf16_f32 v96, v80, v81\n\t" \
  "v_exp_f32 v84, v84\n\t" \
  "v_exp_f32 v85, v85\n\t" \
  "v_cvt_pk_bf16_f32 v97, v82, v83\n\t" \
  "v_mfma_f32_32x32x16_bf16 %[o], v[112:115], v[100:103], %[o]\n\t" \
  "ds_read_b128 v[112:115], v126 offset:4608\n\t" \
  "v_exp_f32 v86, v86\n\t" \
  "v_exp_f32 v87, v87\n\t" \
  "v_cvt_pk_bf16_f32 v98, v84, v85\n\t" \
  "v_exp_f32 v88, v88\n\t" \
  "s_setprio 3\n\t" \
  "v_exp_f32 v89, v89\n\t" \
  "global_load_lds_dwordx4 %[goff], %[gb] offset:1024\n\t" \
  "v_cvt_pk_bf16_f32 v99, v86, v87\n\t" \
  "v_exp_f32 v90, v90\n\t" \
  "s_setprio 1\n\t" \
  "v_exp_f32 v91, v91\n\t" \
  "v_cvt_pk_bf16_f32 v100, v88, v89\n\t" \
  "v_mfma_f32_32x32x16_bf16 %[o], v[116:119], v[96:99], %[o]\n\t" \
  "v_exp_f32 v92, v92\n\t" \
  "v_exp_f32 v93, v93\n\t" \
  "v_cvt_pk_bf16_f32 v101, v90, v91\n\t" \
  "v_exp_f32 v94, v94\n\t" \
  "v_exp_f32 v95, v95\n\t" \
  "v_cvt_pk_bf16_f32 v102, v92, v93\n\t" \
  "s_waitcnt lgkmcnt(1)\n\t" \
  "v_exp_f32 v64, v64\n\t" \
  "v_exp_f32 v65, v65\n\t" \
  "v_cvt_pk_bf16_f32 v103, v94, v95\n\t" \
  "v_mfma_f32_32x32x16_bf16 v[80:95], v[104:107], %[q], %[negm]\n\t" \
  "ds_read_b128 v[104:107], v124 offset:6144\n\t" \
  "v_exp_f32 v66, v66\n\t" \
  "ds_read_b128 v[116:119], v126 offset:5120\n\t" \
  "v_exp_f32 v67, v67\n\t" \
  "s_setprio 1\n\t" \
  "v_cvt_pk_bf16_f32 v96, v64, v65\n\t" \
  "v_exp_f32 v68, v68\n\t" \
  "v_exp_f32 v69, v69\n\t" \
  "s_add_u32 %[sc], %[sc], 0x4000\n\t" \
  "v_cvt_pk_bf16_f32 v97, v66, v67\n\t" \
  "v_mfma_f32_32x32x16_bf16 %[o], v[120:123], v[100:103], %[o]\n\t" \
  "ds_read_b128 v[120:123], v126 offset:5632\n\t" \
  "v_exp_f32 v70, v70\n\t" \
  "v_exp_f32 v71, v71\n\t" \
  "s_and_b32 %[sc], %[sc], 0xc000\n\t" \
  "v_cvt_pk_bf16_f32 v98, v68, v69\n\t" \
  "v_exp_f32 v72, v72\n\t" \
  "v_exp_f32 v73, v73\n\t" \
  "v_add_u32 %[goff], 0x4000, %[goff]\n\t" \
  "v_cvt_pk_bf16_f32 v99, v70, v71\n\t" \
  "v_exp_f32 v74, v74\n\t" \
  "v_exp_f32 v75, v75\n\t" \
  "v_and_b32 %[goff], 0x1ffff, %[goff]\n\t" \
  "v_cvt_pk_bf16_f32 v100, v72, v73\n\t" \
  "v_mfma_f32_32x32x16_bf16 %[o], v[108:111], v[96:99], %[o]\n\t" \
  "v_exp_f32 v76, v76\n\t" \
  "v_exp_f32 v77, v77\n\t" \
  "v_cvt_pk_bf16_f32 v101, v74, v75\n\t" \
  "v_exp_f32 v78, v78\n\t" \
  "v_exp_f32 v79, v79\n\t" \
  "v_cvt_pk_bf16_f32 v102, v76, v77\n\t" \
  "s_waitcnt lgkmcnt(1)\n\t" \
  "v_exp_f32 v80, v80\n\t" \
  "v_exp_f32 v81, v81\n\t" \
  "v_cvt_pk_bf16_f32 v103, v78, v79\n\t" \
  "v_mfma_f32_32x32x16_bf16 v[64:79], v[104:107], %[q], %[negm]\n\t" \
  "ds_read_b128 v[104:107], v124 offset:7168\n\t" \
  "v_exp_f32 v82, v82\n\t" \
  "ds_read_b128 v[108:111], v126 offset:6144\n\t" \
  "v_exp_f32 v83, v83\n\t" \
  "v_cvt_pk_bf16_f32 v96, v80, v81\n\t" \
  "v_exp_f32 v84, v84\n\t" \
  "v_exp_f32 v85, v85\n\t" \
  "v_add_u32 v125, %[sc], %[kl]\n\t" \
  "v_cvt_pk_bf16_f32 v97, v82, v83\n\t" \
  "v_mfma_f32_32x32x16_bf16 %[o], v[112:115], v[100:103], %[o]\n\t" \
  "ds_read_b128 v[112:115], v126 offset:6656\n\t" \
  "v_exp_f32 v86, v86\n\t" \
  "v_exp_f32 v87, v87\n\t" \
  "v_and_b32 v127, %[sc], %[dm]\n\t" \
  "v_cvt_pk_bf16_f32 v98, v84, v85\n\t" \
  "v_exp_f32 v88, v88\n\t" \
  "v_exp_f32 v89, v89\n\t" \
  "v_add_u32 v127, v127, %[vb]\n\t" \
  "v_cvt_pk_bf16_f32 v99, v86, v87\n\t" \
  "v_exp_f32 v90, v90\n\t" \
  "v_exp_f32 v91, v91\n\t" \
  "v_cvt_pk_bf16_f32 v100, v88, v89\n\t" \
  "v_mfma_f32_32x32x16_bf16 %[o], v[116:119], v[96:99], %[o]\n\t" \
  "v_exp_f32 v92, v92\n\t" \
  "v_exp_f32 v93, v93\n\t" \
  "v_cvt_pk_bf16_f32 v101, v90, v91\n\t" \
  "v_exp_f32 v94, v94\n\t" \
  "v_exp_f32 v95, v95\n\t" \
  "v_cvt_pk_bf16_f32 v102, v92, v93\n\t" \
  "s_waitcnt vmcnt(2) lgkmcnt(1)\n\t" \
  "s_barrier\n\t" \
  "v_exp_f32 v64, v64\n\t" \
  "v_exp_f32 v65, v65\n\t" \
  "v_cvt_pk_bf16_f32 v103, v94, v95\n\t" \
  "v_mfma_f32_32x32x16_bf16 v[80:95], v[104:107], %[q], %[negm]\n\t" \
  "ds_read_b128 v[104:107], v125 offset:0\n\t" \
  "v_exp_f32 v66, v66\n\t" \
  "ds_read_b128 v[116:119], v126 offset:7168\n\t" \
  "v_exp_f32 v67, v67\n\t" \
  "s_setprio 2\n\t" \
  "v_cvt_pk_bf16_f32 v96, v64, v65\n\t" \
  "v_exp_f32 v68, v68\n\t" \
  "v_exp_f32 v69, v69\n\t" \
  "v_cvt_pk_bf16_f32 v97, v66, v67\n\t" \
  "v_mfma_f32_32x32x16_bf16 %[o], v[120:123], v[100:103], %[o]\n\t" \
  "ds_read_b128 v[120:123], v126 offset:7680\n\t" \
  "v_exp_f32 v70, v70\n\t" \
  "v_exp_f32 v71, v71\n\t" \
  "v_cvt_pk_bf16_f32 v98, v68, v69\n\t" \
  "v_exp_f32 v72, v72\n\t" \
  "v_exp_f32 v73, v73\n\t" \
  "v_cvt_pk_bf16_f32 v99, v70, v71\n\t" \
  "v_exp_f32 v74, v74\n\t" \
  "v_exp_f32 v75, v75\n\t" \
  "v_cvt_pk_bf16_f32 v100, v72, v73\n\t" \
  "v_mfma_f32_32x32x16_bf16 %[o], v[108:111], v[96:99], %[o]\n\t" \
  "v_exp_f32 v76, v76\n\t" \
  "s_cmp_lg_u32 %[c], 7\n\t" \
  "v_exp_f32 v77, v77\n\t" \
  "s_cbranch_scc1 .Lnh%=\n\t" \
  "s_mov_b64 exec, 1\n\t" \
  "global_store_dword %[l16], %[vb], %[hp] sc1\n\t" \
  "s_mov_b64 exec, -1\n\t" \
  ".Lnh%=:\n\t" \
  "v_cvt_pk_bf16_f32 v101, v74, v75\n\t" \
  "v_exp_f32 v78, v78\n\t" \
  "v_exp_f32 v79, v79\n\t" \
  "v_cvt_pk_bf16_f32 v102, v76, v77\n\t" \
  "s_waitcnt lgkmcnt(1)\n\t" \
  "v_exp_f32 v80, v80\n\t" \
  "v_exp_f32 v81, v81\n\t" \
  "v_cvt_pk_bf16_f32 v103, v78, v79\n\t" \
  "v_mfma_f32_32x32x16_bf16 v[64:79], v[104:107], %[q], %[negm]\n\t" \
  "ds_read_b128 v[104:107], v125 offset:1024\n\t" \
  "v_exp_f32 v82, v82\n\t" \
  "ds_read_b128 v[108:111], v127 offset:0\n\t" \
  "v_exp_f32 v83, v83\n\t" \
  "v_cvt_pk_bf16_f32 v96, v80, v81\n\t" \
  "v_exp_f32 v84, v84\n\t" \
  "v_exp_f32 v85, v85\n\t" \
  "v_cvt_pk_bf16_f32 v97, v82, v83\n\t" \
  "v_mfma_f32_32x32x16_bf16 %[o], v[112:115], v[100:103], %[o]\n\t" \
  "ds_read_b128 v[112:115], v127 offset:512\n\t" \
  "v_exp_f32 v86, v86\n\t" \
  "v_exp_f32 v87, v87\n\t" \
  "v_cvt_pk_bf16_f32 v98, v84, v85\n\t" \
  "v_exp_f32 v88, v88\n\t" \
  "v_exp_f32 v89, v89\n\t" \
  "v_mov_b32 v124, v125\n\t" \
  "v_cvt_pk_bf16_f32 v99, v86, v87\n\t" \
  "v_exp_f32 v90, v90\n\t" \
  "v_exp_f32 v91, v91\n\t" \
  "v_mov_b32 v126, v127\n\t" \
  "v_cvt_pk_bf16_f32 v100, v88, v89\n\t" \
  "v_mfma_f32_32x32x16_bf16 %[o], v[116:119], v[96:99], %[o]\n\t" \
  "v_exp_f32 v92, v92\n\t" \
  "v_exp_f32 v93, v93\n\t" \
  "s_add_u32 %[c], %[c], 1\n\t" \
  "v_cvt_pk_bf16_f32 v101, v90, v91\n\t" \
  "v_exp_f32 v94, v94\n\t" \
  "s_cmp_lt_u32 %[c], 8\n\t" \
  "v_exp_f32 v95, v95\n\t" \
  "v_cvt_pk_bf16_f32 v102, v92, v93\n\t" \
  "s_cbranch_scc1 .Lloop%=\n\t" \
  "s_nop 0\n\t" \
  "v_cvt_pk_bf16_f32 v103, v94, v95\n\t" \
  "s_waitcnt lgkmcnt(0)\n\t" \
  "s_nop 1\n\t" \
  "v_mfma_f32_32x32x16_bf16 %[o], v[120:123], v[100:103], %[o]\n\t" \
  "s_mov_b32 m0, %[sm0]\n\t" \
  "s_setprio 0\n\t" \
  "s_nop 15\n\t" \
  ""
#define ATTN_CLOBBERS "v64","v65","v66","v67","v68","v69","v70","v71","v72","v73","v74","v75","v76","v77","v78","v79","v80","v81","v82","v83","v84","v85","v86","v87","v88","v89","v90","v91","v92","v93","v94","v95","v96","v97","v98","v99","v100","v101","v102","v103","v104","v105","v106","v107","v108","v109","v110","v111","v112","v113","v114","v115","v116","v117","v118","v119","v120","v121","v122","v123","v124","v125","v126","v127"
#define G_Z_ASM \
  "s_nop 4\n\t" \
  "s_mov_b64 s[90:91], %[wp]\n\t" \
  "v_mov_b32 v56, 1\n\t" \
  "v_mov_b32 v57, 0\n\t" \
  "s_barrier\n\t" \
  "global_load_dwordx4 v[64:67], %[l16], s[90:91] offset:-4096\n\t" \
  "global_load_dwordx4 v[68:71], %[l16], s[90:91] offset:-3072\n\t" \
  "global_load_dwordx4 v[72:75], %[l16], s[90:91] offset:-2048\n\t" \
  "global_load_dwordx4 v[76:79], %[l16], s[90:91] offset:-1024\n\t" \
  "global_load_dwordx4 v[80:83], %[l16], s[90:91] offset:0\n\t" \
  "global_load_dwordx4 v[84:87], %[l16], s[90:91] offset:1024\n\t" \
  "s_waitcnt vmcnt(6)\n\t" \
  "s_barrier\n\t" \
  "s_cmp_lg_u32 %[wid], 0\n\t" \
  "s_cbranch_scc1 .Lzf%=\n\t" \
  "s_mov_b64 exec, 1\n\t" \
  "global_store_dword v57, v56, %[zf] sc1\n\t" \
  "s_mov_b64 exec, -1\n\t" \
  "s_nop 0\n\t" \
  ".Lzf%=:\n\t" \
  "ds_read_b128 v[48:51], %[la] offset:0\n\t" \
  "ds_read_b128 v[52:55], %[lc] offset:0\n\t" \
  "ds_read_b128 v[56:59], %[la] offset:1024\n\t" \
  "ds_read_b128 v[60:63], %[lc] offset:1024\n\t" \
  "s_waitcnt vmcnt(5) lgkmcnt(3)\n\t" \
  "v_mfma_f32_32x32x16_bf16 %[z0], v[64:67], v[48:51], 0\n\t" \
  "s_waitcnt lgkmcnt(2)\n\t" \
  "v_mfma_f32_32x32x16_bf16 %[z1], v[64:67], v[52:55], 0\n\t" \
  "ds_read_b128 v[48:51], %[la] offset:2048\n\t" \
  "ds_read_b128 v[52:55], %[lc] offset:2048\n\t" \
  "global_load_dwordx4 v[88:91], %[l16], s[90:91] offset:2048\n\t" \
  "global_load_dwordx4 v[92:95], %[l16], s[90:91] offset:3072\n\t" \
  "s_waitcnt vmcnt(6) lgkmcnt(3)\n\t" \
  "v_mfma_f32_32x32x16_bf16 %[z0], v[68:71], v[56:59], %[z0]\n\t" \
  "s_waitcnt lgkmcnt(2)\n\t" \
  "v_mfma_f32_32x32x16_bf16 %[z1], v[68:71], v[60:63], %[z1]\n\t" \
  "ds_read_b128 v[56:59], %[la] offset:3072\n\t" \
  "ds_read_b128 v[60:63], %[lc] offset:3072\n\t" \
  "s_add_u32 s90, s90, 0x2000\n\t" \
  "s_addc_u32 s91, s91, 0\n\t" \
  "global_load_dwordx4 v[96:99], %[l16], s[90:91] offset:-4096\n\t" \
  "global_load_dwordx4 v[100:103], %[l16], s[90:91] offset:-3072\n\t" \
  "s_waitcnt vmcnt(7) lgkmcnt(3)\n\t" \
  "v_mfma_f32_32x32x16_bf16 %[z0], v[72:75], v[48:51], %[z0]\n\t" \
  "s_waitcnt lgkmcnt(2)\n\t" \
  "v_mfma_f32_32x32x16_bf16 %[z1], v[72:75], v[52:55], %[z1]\n\t" \
  "ds_read_b128 v[48:51], %[la] offset:4096\n\t" \
  "ds_read_b128 v[52:55], %[lc] offset:4096\n\t" \
  "global_load_dwordx4 v[104:107], %[l16], s[90:91] offset:-2048\n\t" \
  "global_load_dwordx4 v[108:111], %[l16], s[90:91] offset:-1024\n\t" \
  "s_waitcnt vmcnt(8) lgkmcnt(3)\n\t" \
  "v_mfma_f32_32x32x16_bf16 %[z0], v[76:79], v[56:59], %[z0]\n\t" \
  "s_waitcnt lgkmcnt(2)\n\t" \
  "v_mfma_f32_32x32x16_bf16 %[z1], v[76:79], v[60:63], %[z1]\n\t" \
  "ds_read_b128 v[56:59], %[la] offset:5120\n\t" \
  "ds_read_b128 v[60:63], %[lc] offset:5120\n\t" \
  "global_load_dwordx4 v[112:115], %[l16], s[90:91] offset:0\n\t" \
  "global_load_dwordx4 v[116:119], %[l16], s[90:91] offset:1024\n\t" \
  "s_waitcnt vmcnt(9) lgkmcnt(3)\n\t" \
  "v_mfma_f32_32x32x16_bf16 %[z0], v[80:83], v[48:51], %[z0]\n\t" \
  "s_waitcnt lgkmcnt(2)\n\t" \
  "v_mfma_f32_32x32x16_bf16 %[z1], v[80:83], v[52:55], %[z1]\n\t" \
  "ds_read_b128 v[48:51], %[la] offset:6144\n\t" \
  "ds_read_b128 v[52:55], %[lc] offset:6144\n\t" \
  "global_load_dwordx4 v[120:123], %[l16], s[90:91] offset:2048\n\t" \
  "global_load_dwordx4 v[124:127], %[l16], s[90:91] offset:3072\n\t" \
  "s_waitcnt vmcnt(10) lgkmcnt(3)\n\t" \
  "v_mfma_f32_32x32x16_bf16 %[z0], v[84:87], v[56:59], %[z0]\n\t" \
  "s_waitcnt lgkmcnt(2)\n\t" \
  "v_mfma_f32_32x32x16_bf16 %[z1], v[84:87], v[60:63], %[z1]\n\t" \
  "ds_read_b128 v[56:59], %[la] offset:7168\n\t" \
  "ds_read_b128 v[60:63], %[lc] offset:7168\n\t" \
  "s_waitcnt vmcnt(9) lgkmcnt(3)\n\t" \
  "v_mfma_f32_32x32x16_bf16 %[z0], v[88:91], v[48:51], %[z0]\n\t" \
  "s_waitcnt lgkmcnt(2)\n\t" \
  "v_mfma_f32_32x32x16_bf16 %[z1], v[88:91], v[52:55], %[z1]\n\t" \
  "ds_read_b128 v[48:51], %[la] offset:32768\n\t" \
  "ds_read_b128 v[52:55], %[lc] offset:32768\n\t" \
  "s_waitcnt vmcnt(8) lgkmcnt(3)\n\t" \
  "v_mfma_f32_32x32x16_bf16 %[z0], v[92:95], v[56:59], %[z0]\n\t" \
  "s_waitcnt lgkmcnt(2)\n\t" \
  "v_mfma_f32_32x32x16_bf16 %[z1], v[92:95], v[60:63], %[z1]\n\t" \
  "ds_read_b128 v[56:59], %[la] offset:33792\n\t" \
  "ds_read_b128 v[60:63], %[lc] offset:33792\n\t" \
  "s_barrier\n\t" \
  "s_add_u32 m0, %[mb], 0\n\t" \
  "s_add_u32 %[t0], %[rot], 0\n\t" \
  "s_and_b32 %[t0], %[t0], 15\n\t" \
  "s_lshl_b32 %[t0], %[t0], 8\n\t" \
  "s_add_u32 s92, %[wl], %[t0]\n\t" \
  "s_addc_u32 s93, %[wh], 0\n\t" \
  "global_load_lds_dwordx4 %[av0], s[92:93]\n\t" \
  "global_load_lds_dwordx4 %[av1], s[92:93] offset:1024\n\t" \
  "s_add_u32 m0, %[mb], 16384\n\t" \
  "s_add_u32 %[t0], %[rot], 1\n\t" \
  "s_and_b32 %[t0], %[t0], 15\n\t" \
  "s_lshl_b32 %[t0], %[t0], 8\n\t" \
  "s_add_u32 s92, %[wl], %[t0]\n\t" \
  "s_addc_u32 s93, %[wh], 0\n\t" \
  "global_load_lds_dwordx4 %[av0], s[92:93]\n\t" \
  "global_load_lds_dwordx4 %[av1], s[92:93] offset:1024\n\t" \
  "s_waitcnt vmcnt(11) lgkmcnt(3)\n\t" \
  "v_mfma_f32_32x32x16_bf16 %[z0], v[96:99], v[48:51], %[z0]\n\t" \
  "s_waitcnt lgkmcnt(2)\n\t" \
  "v_mfma_f32_32x32x16_bf16 %[z1], v[96:99], v[52:55], %[z1]\n\t" \
  "ds_read_b128 v[48:51], %[la] offset:34816\n\t" \
  "ds_read_b128 v[52:55], %[lc] offset:34816\n\t" \
  "s_waitcnt vmcnt(10) lgkmcnt(3)\n\t" \
  "v_mfma_f32_32x32x16_bf16 %[z0], v[100:103], v[56:59], %[z0]\n\t" \
  "s_waitcnt lgkmcnt(2)\n\t" \
  "v_mfma_f32_32x32x16_bf16 %[z1], v[100:103], v[60:63], %[z1]\n\t" \
  "ds_read_b128 v[56:59], %[la] offset:35840\n\t" \
  "ds_read_b128 v[60:63], %[lc] offset:35840\n\t" \
  "s_waitcnt vmcnt(9) lgkmcnt(3)\n\t" \
  "v_mfma_f32_32x32x16_bf16 %[z0], v[104:107], v[48:51], %[z0]\n\t" \
  "s_waitcnt lgkmcnt(2)\n\t" \
  "v_mfma_f32_32x32x16_bf16 %[z1], v[104:107], v[52:55], %[z1]\n\t" \
  "ds_read_b128 v[48:51], %[la] offset:36864\n\t" \
  "ds_read_b128 v[52:55], %[lc] offset:36864\n\t" \
  "s_waitcnt vmcnt(8) lgkmcnt(3)\n\t" \
  "v_mfma_f32_32x32x16_bf16 %[z0], v[108:111], v[56:59], %[z0]\n\t" \
  "s_waitcnt lgkmcnt(2)\n\t" \
  "v_mfma_f32_32x32x16_bf16 %[z1], v[108:111], v[60:63], %[z1]\n\t" \
  "ds_read_b128 v[56:59], %[la] offset:37888\n\t" \
  "ds_read_b128 v[60:63], %[lc] offset:37888\n\t" \
  "s_waitcnt vmcnt(7) lgkmcnt(3)\n\t" \
  "v_mfma_f32_32x32x16_bf16 %[z0], v[112:115], v[48:51], %[z0]\n\t" \
  "s_waitcnt lgkmcnt(2)\n\t" \
  "v_mfma_f32_32x32x16_bf16 %[z1], v[112:115], v[52:55], %[z1]\n\t" \
  "ds_read_b128 v[48:51], %[la] offset:38912\n\t" \
  "ds_read_b128 v[52:55], %[lc] offset:38912\n\t" \
  "s_waitcnt vmcnt(6) lgkmcnt(3)\n\t" \
  "v_mfma_f32_32x32x16_bf16 %[z0], v[116:119], v[56:59], %[z0]\n\t" \
  "s_waitcnt lgkmcnt(2)\n\t" \
  "v_mfma_f32_32x32x16_bf16 %[z1], v[116:119], v[60:63], %[z1]\n\t" \
  "ds_read_b128 v[56:59], %[la] offset:39936\n\t" \
  "ds_read_b128 v[60:63], %[lc] offset:39936\n\t" \
  "s_waitcnt vmcnt(5) lgkmcnt(3)\n\t" \
  "v_mfma_f32_32x32x16_bf16 %[z0], v[120:123], v[48:51], %[z0]\n\t" \
  "s_waitcnt lgkmcnt(2)\n\t" \
  "v_mfma_f32_32x32x16_bf16 %[z1], v[120:123], v[52:55], %[z1]\n\t" \
  "s_waitcnt vmcnt(4) lgkmcnt(1)\n\t" \
  "v_mfma_f32_32x32x16_bf16 %[z0], v[124:127], v[56:59], %[z0]\n\t" \
  "s_waitcnt lgkmcnt(0)\n\t" \
  "v_mfma_f32_32x32x16_bf16 %[z1], v[124:127], v[60:63], %[z1]\n\t" \
  "s_barrier\n\t" \
  "s_add_u32 m0, %[mb], 32768\n\t" \
  "s_add_u32 %[t0], %[rot], 2\n\t" \
  "s_and_b32 %[t0], %[t0], 15\n\t" \
  "s_lshl_b32 %[t0], %[t0], 8\n\t" \
  "s_add_u32 s92, %[wl], %[t0]\n\t" \
  "s_addc_u32 s93, %[wh], 0\n\t" \
  "global_load_lds_dwordx4 %[av0], s[92:93]\n\t" \
  "global_load_lds_dwordx4 %[av1], s[92:93] offset:1024\n\t" \
  "s_nop 4\n\t" \
  ""
#define G_S_ASM \
  "s_nop 4\n\t" \
  "s_add_u32 s92, %[rot], 3\n\t" \
  "s_and_b32 s92, s92, 15\n\t" \
  "s_mov_b32 s93, %[rot]\n\t" \
  "s_mov_b32 s95, 0xc000\n\t" \
  "s_cmp_lg_u32 %[wid], 0\n\t" \
  "s_cbranch_scc1 .Lqs%=\n\t" \
  "v_lshrrev_b32 v98, 2, %[l16]\n\t" \
  "v_and_b32 v98, 124, v98\n\t" \
  "s_mov_b32 %[t0], 0x40000\n\t" \
  ".Lps%=:\n\t" \
  "global_load_dword v96, v98, %[fl] sc1\n\t" \
  "s_waitcnt vmcnt(0)\n\t" \
  "v_cmp_eq_u32 vcc, 0, v96\n\t" \
  "s_cbranch_vccz .Lqs%=\n\t" \
  "s_sleep 8\n\t" \
  "s_sub_u32 %[t0], %[t0], 1\n\t" \
  "s_cbranch_scc0 .Lps%=\n\t" \
  ".Lqs%=:\n\t" \
  "s_waitcnt vmcnt(0)\n\t" \
  "s_barrier\n\t" \
  "s_setprio 0\n\t" \
  "s_lshl_b32 %[t0], s93, 13\n\t" \
  "s_add_u32 s96, %[zl], %[t0]\n\t" \
  "s_addc_u32 s97, %[zh], 0\n\t" \
  "s_add_u32 s93, s93, 1\n\t" \
  "s_and_b32 s93, s93, 15\n\t" \
  "global_load_dwordx4 v[64:67], %[l16], s[96:97] offset:0 sc1\n\t" \
  "global_load_dwordx4 v[68:71], %[l16], s[96:97] offset:1024 sc1\n\t" \
  "global_load_dwordx4 v[72:75], %[l16], s[96:97] offset:2048 sc1\n\t" \
  "global_load_dwordx4 v[76:79], %[l16], s[96:97] offset:3072 sc1\n\t" \
  "s_lshl_b32 %[t0], s93, 13\n\t" \
  "s_add_u32 s96, %[zl], %[t0]\n\t" \
  "s_addc_u32 s97, %[zh], 0\n\t" \
  "s_add_u32 s93, s93, 1\n\t" \
  "s_and_b32 s93, s93, 15\n\t" \
  "global_load_dwordx4 v[80:83], %[l16], s[96:97] offset:0 sc1\n\t" \
  "global_load_dwordx4 v[84:87], %[l16], s[96:97] offset:1024 sc1\n\t" \
  "global_load_dwordx4 v[88:91], %[l16], s[96:97] offset:2048 sc1\n\t" \
  "global_load_dwordx4 v[92:95], %[l16], s[96:97] offset:3072 sc1\n\t" \
  "ds_read_b128 v[96:99], %[a0] offset:0\n\t" \
  "ds_read_b128 v[100:103], %[c0] offset:0\n\t" \
  "ds_read_b128 v[104:107], %[a1] offset:0\n\t" \
  "ds_read_b128 v[108:111], %[c1] offset:0\n\t" \
  "ds_read_b128 v[112:115], %[a2] offset:0\n\t" \
  "ds_read_b128 v[116:119], %[c2] offset:0\n\t" \
  "ds_read_b128 v[120:123], %[a3] offset:0\n\t" \
  "ds_read_b128 v[124:127], %[c3] offset:0\n\t" \
  "s_waitcnt vmcnt(7) lgkmcnt(7)\n\t" \
  "v_mfma_f32_32x32x16_bf16 %[g0], v[64:67], v[96:99], 0\n\t" \
  "s_lshl_b32 %[t0], s92, 8\n\t" \
  "s_add_u32 s90, %[wl], %[t0]\n\t" \
  "s_addc_u32 s91, %[wh], 0\n\t" \
  "s_add_u32 m0, s95, %[mb]\n\t" \
  "s_add_u32 s92, s92, 1\n\t" \
  "s_and_b32 s92, s92, 15\n\t" \
  "global_load_lds_dwordx4 %[av0], s[90:91]\n\t" \
  "global_load_lds_dwordx4 %[av1], s[90:91] offset:1024\n\t" \
  "s_add_u32 s95, s95, 0x4000\n\t" \
  "s_and_b32 s95, s95, 0xc000\n\t" \
  "s_lshl_b32 %[t0], s93, 13\n\t" \
  "s_add_u32 s96, %[zl], %[t0]\n\t" \
  "s_addc_u32 s97, %[zh], 0\n\t" \
  "s_add_u32 s93, s93, 1\n\t" \
  "s_and_b32 s93, s93, 15\n\t" \
  "s_waitcnt lgkmcnt(6)\n\t" \
  "v_mfma_f32_32x32x16_bf16 %[g1], v[64:67], v[100:103], 0\n\t" \
  "ds_read_b128 v[96:99], %[a0] offset:16384\n\t" \
  "ds_read_b128 v[100:103], %[c0] offset:16384\n\t" \
  "global_load_dwordx4 v[64:67], %[l16], s[96:97] offset:0 sc1\n\t" \
  "s_waitcnt vmcnt(9) lgkmcnt(7)\n\t" \
  "v_mfma_f32_32x32x16_bf16 %[g0], v[68:71], v[104:107], %[g0]\n\t" \
  "s_waitcnt lgkmcnt(6)\n\t" \
  "v_mfma_f32_32x32x16_bf16 %[g1], v[68:71], v[108:111], %[g1]\n\t" \
  "ds_read_b128 v[104:107], %[a1] offset:16384\n\t" \
  "ds_read_b128 v[108:111], %[c1] offset:16384\n\t" \
  "global_load_dwordx4 v[68:71], %[l16], s[96:97] offset:1024 sc1\n\t" \
  "s_waitcnt vmcnt(9) lgkmcnt(7)\n\t" \
  "v_mfma_f32_32x32x16_bf16 %[g0], v[72:75], v[112:115], %[g0]\n\t" \
  "s_waitcnt lgkmcnt(6)\n\t" \
  "v_mfma_f32_32x32x16_bf16 %[g1], v[72:75], v[116:119], %[g1]\n\t" \
  "ds_read_b128 v[112:115], %[a2] offset:16384\n\t" \
  "ds_read_b128 v[116:119], %[c2] offset:16384\n\t" \
  "global_load_dwordx4 v[72:75], %[l16], s[96:97] offset:2048 sc1\n\t" \
  "s_waitcnt vmcnt(9) lgkmcnt(7)\n\t" \
  "v_mfma_f32_32x32x16_bf16 %[g0], v[76:79], v[120:123], %[g0]\n\t" \
  "s_waitcnt lgkmcnt(6)\n\t" \
  "v_mfma_f32_32x32x16_bf16 %[g1], v[76:79], v[124:127], %[g1]\n\t" \
  "ds_read_b128 v[120:123], %[a3] offset:16384\n\t" \
  "ds_read_b128 v[124:127], %[c3] offset:16384\n\t" \
  "global_load_dwordx4 v[76:79], %[l16], s[96:97] offset:3072 sc1\n\t" \
  "s_barrier\n\t" \
  "s_waitcnt vmcnt(9) lgkmcnt(7)\n\t" \
  "v_mfma_f32_32x32x16_bf16 %[g0], v[80:83], v[96:99], %[g0]\n\t" \
  "s_lshl_b32 %[t0], s92, 8\n\t" \
  "s_add_u32 s90, %[wl], %[t0]\n\t" \
  "s_addc_u32 s91, %[wh], 0\n\t" \
  "s_add_u32 m0, s95, %[mb]\n\t" \
  "s_add_u32 s92, s92, 1\n\t" \
  "s_and_b32 s92, s92, 15\n\t" \
  "global_load_lds_dwordx4 %[av0], s[90:91]\n\t" \
  "global_load_lds_dwordx4 %[av1], s[90:91] offset:1024\n\t" \
  "s_add_u32 s95, s95, 0x4000\n\t" \
  "s_and_b32 s95, s95, 0xc000\n\t" \
  "s_lshl_b32 %[t0], s93, 13\n\t" \
  "s_add_u32 s96, %[zl], %[t0]\n\t" \
  "s_addc_u32 s97, %[zh], 0\n\t" \
  "s_add_u32 s93, s93, 1\n\t" \
  "s_and_b32 s93, s93, 15\n\t" \
  "s_waitcnt lgkmcnt(6)\n\t" \
  "v_mfma_f32_32x32x16_bf16 %[g1], v[80:83], v[100:103], %[g1]\n\t" \
  "ds_read_b128 v[96:99], %[a0] offset:32768\n\t" \
  "ds_read_b128 v[100:103], %[c0] offset:32768\n\t" \
  "global_load_dwordx4 v[80:83], %[l16], s[96:97] offset:0 sc1\n\t" \
  "s_waitcnt vmcnt(11) lgkmcnt(7)\n\t" \
  "v_mfma_f32_32x32x16_bf16 %[g0], v[84:87], v[104:107], %[g0]\n\t" \
  "s_waitcnt lgkmcnt(6)\n\t" \
  "v_mfma_f32_32x32x16_bf16 %[g1], v[84:87], v[108:111], %[g1]\n\t" \
  "ds_read_b128 v[104:107], %[a1] offset:32768\n\t" \
  "ds_read_b128 v[108:111], %[c1] offset:32768\n\t" \
  "global_load_dwordx4 v[84:87], %[l16], s[96:97] offset:1024 sc1\n\t" \
  "s_waitcnt vmcnt(11) lgkmcnt(7)\n\t" \
  "v_mfma_f32_32x32x16_bf16 %[g0], v[88:91], v[112:115], %[g0]\n\t" \
  "s_waitcnt lgkmcnt(6)\n\t" \
  "v_mfma_f32_32x32x16_bf16 %[g1], v[88:91], v[116:119], %[g1]\n\t" \
  "ds_read_b128 v[112:115], %[a2] offset:32768\n\t" \
  "ds_read_b128 v[116:119], %[c2] offset:32768\n\t" \
  "global_load_dwordx4 v[88:91], %[l16], s[96:97] offset:2048 sc1\n\t" \
  "s_waitcnt vmcnt(11) lgkmcnt(7)\n\t" \
  "v_mfma_f32_32x32x16_bf16 %[g0], v[92:95], v[120:123], %[g0]\n\t" \
  "s_waitcnt lgkmcnt(6)\n\t" \
  "v_mfma_f32_32x32x16_bf16 %[g1], v[92:95], v[124:127], %[g1]\n\t" \
  "ds_read_b128 v[120:123], %[a3] offset:32768\n\t" \
  "ds_read_b128 v[124:127], %[c3] offset:32768\n\t" \
  "global_load_dwordx4 v[92:95], %[l16], s[96:97] offset:3072 sc1\n\t" \
  "s_waitcnt vmcnt(10)\n\t" \
  "s_barrier\n\t" \
  "s_waitcnt vmcnt(9) lgkmcnt(7)\n\t" \
  "v_mfma_f32_32x32x16_bf16 %[g0], v[64:67], v[96:99], %[g0]\n\t" \
  "s_lshl_b32 %[t0], s92, 8\n\t" \
  "s_add_u32 s90, %[wl], %[t0]\n\t" \
  "s_addc_u32 s91, %[wh], 0\n\t" \
  "s_add_u32 m0, s95, %[mb]\n\t" \
  "s_add_u32 s92, s92, 1\n\t" \
  "s_and_b32 s92, s92, 15\n\t" \
  "global_load_lds_dwordx4 %[av0], s[90:91]\n\t" \
  "global_load_lds_dwordx4 %[av1], s[90:91] offset:1024\n\t" \
  "s_add_u32 s95, s95, 0x4000\n\t" \
  "s_and_b32 s95, s95, 0xc000\n\t" \
  "s_lshl_b32 %[t0], s93, 13\n\t" \
  "s_add_u32 s96, %[zl], %[t0]\n\t" \
  "s_addc_u32 s97, %[zh], 0\n\t" \
  "s_add_u32 s93, s93, 1\n\t" \
  "s_and_b32 s93, s93, 15\n\t" \
  "s_waitcnt lgkmcnt(6)\n\t" \
  "v_mfma_f32_32x32x16_bf16 %[g1], v[64:67], v[100:103], %[g1]\n\t" \
  "ds_read_b128 v[96:99], %[a0] offset:49152\n\t" \
  "ds_read_b128 v[100:103], %[c0] offset:49152\n\t" \
  "global_load_dwordx4 v[64:67], %[l16], s[96:97] offset:0 sc1\n\t" \
  "s_waitcnt vmcnt(11) lgkmcnt(7)\n\t" \
  "v_mfma_f32_32x32x16_bf16 %[g0], v[68:71], v[104:107], %[g0]\n\t" \
  "s_waitcnt lgkmcnt(6)\n\t" \
  "v_mfma_f32_32x32x16_bf16 %[g1], v[68:71], v[108:111], %[g1]\n\t" \
  "ds_read_b128 v[104:107], %[a1] offset:49152\n\t" \
  "ds_read_b128 v[108:111], %[c1] offset:49152\n\t" \
  "global_load_dwordx4 v[68:71], %[l16], s[96:97] offset:1024 sc1\n\t" \
  "s_waitcnt vmcnt(11) lgkmcnt(7)\n\t" \
  "v_mfma_f32_32x32x16_bf16 %[g0], v[72:75], v[112:115], %[g0]\n\t" \
  "s_waitcnt lgkmcnt(6)\n\t" \
  "v_mfma_f32_32x32x16_bf16 %[g1], v[72:75], v[116:119], %[g1]\n\t" \
  "ds_read_b128 v[112:115], %[a2] offset:49152\n\t" \
  "ds_read_b128 v[116:119], %[c2] offset:49152\n\t" \
  "global_load_dwordx4 v[72:75], %[l16], s[96:97] offset:2048 sc1\n\t" \
  "s_waitcnt vmcnt(11) lgkmcnt(7)\n\t" \
  "v_mfma_f32_32x32x16_bf16 %[g0], v[76:79], v[120:123], %[g0]\n\t" \
  "s_waitcnt lgkmcnt(6)\n\t" \
  "v_mfma_f32_32x32x16_bf16 %[g1], v[76:79], v[124:127], %[g1]\n\t" \
  "ds_read_b128 v[120:123], %[a3] offset:49152\n\t" \
  "ds_read_b128 v[124:127], %[c3] offset:49152\n\t" \
  "global_load_dwordx4 v[76:79], %[l16], s[96:97] offset:3072 sc1\n\t" \
  "s_waitcnt vmcnt(10)\n\t" \
  "s_barrier\n\t" \
  "s_waitcnt vmcnt(9) lgkmcnt(7)\n\t" \
  "v_mfma_f32_32x32x16_bf16 %[g0], v[80:83], v[96:99], %[g0]\n\t" \
  "s_lshl_b32 %[t0], s92, 8\n\t" \
  "s_add_u32 s90, %[wl], %[t0]\n\t" \
  "s_addc_u32 s91, %[wh], 0\n\t" \
  "s_add_u32 m0, s95, %[mb]\n\t" \
  "s_add_u32 s92, s92, 1\n\t" \
  "s_and_b32 s92, s92, 15\n\t" \
  "global_load_lds_dwordx4 %[av0], s[90:91]\n\t" \
  "global_load_lds_dwordx4 %[av1], s[90:91] offset:1024\n\t" \
  "s_add_u32 s95, s95, 0x4000\n\t" \
  "s_and_b32 s95, s95, 0xc000\n\t" \
  "s_lshl_b32 %[t0], s93, 13\n\t" \
  "s_add_u32 s96, %[zl], %[t0]\n\t" \
  "s_addc_u32 s97, %[zh], 0\n\t" \
  "s_add_u32 s93, s93, 1\n\t" \
  "s_and_b32 s93, s93, 15\n\t" \
  "s_waitcnt lgkmcnt(6)\n\t" \
  "v_mfma_f32_32x32x16_bf16 %[g1], v[80:83], v[100:103], %[g1]\n\t" \
  "ds_read_b128 v[96:99], %[a0] offset:0\n\t" \
  "ds_read_b128 v[100:103], %[c0] offset:0\n\t" \
  "global_load_dwordx4 v[80:83], %[l16], s[96:97] offset:0 sc1\n\t" \
  "s_waitcnt vmcnt(11) lgkmcnt(7)\n\t" \
  "v_mfma_f32_32x32x16_bf16 %[g0], v[84:87], v[104:107], %[g0]\n\t" \
  "s_waitcnt lgkmcnt(6)\n\t" \
  "v_mfma_f32_32x32x16_bf16 %[g1], v[84:87], v[108:111], %[g1]\n\t" \
  "ds_read_b128 v[104:107], %[a1] offset:0\n\t" \
  "ds_read_b128 v[108:111], %[c1] offset:0\n\t" \
  "global_load_dwordx4 v[84:87], %[l16], s[96:97] offset:1024 sc1\n\t" \
  "s_waitcnt vmcnt(11) lgkmcnt(7)\n\t" \
  "v_mfma_f32_32x32x16_bf16 %[g0], v[88:91], v[112:115], %[g0]\n\t" \
  "s_waitcnt lgkmcnt(6)\n\t" \
  "v_mfma_f32_32x32x16_bf16 %[g1], v[88:91], v[116:119], %[g1]\n\t" \
  "ds_read_b128 v[112:115], %[a2] offset:0\n\t" \
  "ds_read_b128 v[116:119], %[c2] offset:0\n\t" \
  "global_load_dwordx4 v[88:91], %[l16], s[96:97] offset:2048 sc1\n\t" \
  "s_waitcnt vmcnt(11) lgkmcnt(7)\n\t" \
  "v_mfma_f32_32x32x16_bf16 %[g0], v[92:95], v[120:123], %[g0]\n\t" \
  "s_waitcnt lgkmcnt(6)\n\t" \
  "v_mfma_f32_32x32x16_bf16 %[g1], v[92:95], v[124:127], %[g1]\n\t" \
  "ds_read_b128 v[120:123], %[a3] offset:0\n\t" \
  "ds_read_b128 v[124:127], %[c3] offset:0\n\t" \
  "global_load_dwordx4 v[92:95], %[l16], s[96:97] offset:3072 sc1\n\t" \
  "s_waitcnt vmcnt(10)\n\t" \
  "s_barrier\n\t" \
  "s_mov_b32 s98, 2\n\t" \
  ".Ls%=:\n\t" \
  "s_waitcnt vmcnt(9) lgkmcnt(7)\n\t" \
  "v_mfma_f32_32x32x16_bf16 %[g0], v[64:67], v[96:99], %[g0]\n\t" \
  "s_lshl_b32 %[t0], s92, 8\n\t" \
  "s_add_u32 s90, %[wl], %[t0]\n\t" \
  "s_addc_u32 s91, %[wh], 0\n\t" \
  "s_add_u32 m0, s95, %[mb]\n\t" \
  "s_add_u32 s92, s92, 1\n\t" \
  "s_and_b32 s92, s92, 15\n\t" \
  "global_load_lds_dwordx4 %[av0], s[90:91]\n\t" \
  "global_load_lds_dwordx4 %[av1], s[90:91] offset:1024\n\t" \
  "s_add_u32 s95, s95, 0x4000\n\t" \
  "s_and_b32 s95, s95, 0xc000\n\t" \
  "s_lshl_b32 %[t0], s93, 13\n\t" \
  "s_add_u32 s96, %[zl], %[t0]\n\t" \
  "s_addc_u32 s97, %[zh], 0\n\t" \
  "s_add_u32 s93, s93, 1\n\t" \
  "s_and_b32 s93, s93, 15\n\t" \
  "s_waitcnt lgkmcnt(6)\n\t" \
  "v_mfma_f32_32x32x16_bf16 %[g1], v[64:67], v[100:103], %[g1]\n\t" \
  "ds_read_b128 v[96:99], %[a0] offset:16384\n\t" \
  "ds_read_b128 v[100:103], %[c0] offset:16384\n\t" \
  "global_load_dwordx4 v[64:67], %[l16], s[96:97] offset:0 sc1\n\t" \
  "s_waitcnt vmcnt(11) lgkmcnt(7)\n\t" \
  "v_mfma_f32_32x32x16_bf16 %[g0], v[68:71], v[104:107], %[g0]\n\t" \
  "s_waitcnt lgkmcnt(6)\n\t" \
  "v_mfma_f32_32x32x16_bf16 %[g1], v[68:71], v[108:111], %[g1]\n\t" \
  "ds_read_b128 v[104:107], %[a1] offset:16384\n\t" \
  "ds_read_b128 v[108:111], %[c1] offset:16384\n\t" \
  "global_load_dwordx4 v[68:71], %[l16], s[96:97] offset:1024 sc1\n\t" \
  "s_waitcnt vmcnt(11) lgkmcnt(7)\n\t" \
  "v_mfma_f32_32x32x16_bf16 %[g0], v[72:75], v[112:115], %[g0]\n\t" \
  "s_waitcnt lgkmcnt(6)\n\t" \
  "v_mfma_f32_32x32x16_bf16 %[g1], v[72:75], v[116:119], %[g1]\n\t" \
  "ds_read_b128 v[112:115], %[a2] offset:16384\n\t" \
  "ds_read_b128 v[116:119], %[c2] offset:16384\n\t" \
  "global_load_dwordx4 v[72:75], %[l16], s[96:97] offset:2048 sc1\n\t" \
  "s_waitcnt vmcnt(11) lgkmcnt(7)\n\t" \
  "v_mfma_f32_32x32x16_bf16 %[g0], v[76:79], v[120:123], %[g0]\n\t" \
  "s_waitcnt lgkmcnt(6)\n\t" \
  "v_mfma_f32_32x32x16_bf16 %[g1], v[76:79], v[124:127], %[g1]\n\t" \
  "ds_read_b128 v[120:123], %[a3] offset:16384\n\t" \
  "ds_read_b128 v[124:127], %[c3] offset:16384\n\t" \
  "global_load_dwordx4 v[76:79], %[l16], s[96:97] offset:3072 sc1\n\t" \
  "s_waitcnt vmcnt(10)\n\t" \
  "s_barrier\n\t" \
  "s_waitcnt vmcnt(9) lgkmcnt(7)\n\t" \
  "v_mfma_f32_32x32x16_bf16 %[g0], v[80:83], v[96:99], %[g0]\n\t" \
  "s_lshl_b32 %[t0], s92, 8\n\t" \
  "s_add_u32 s90, %[wl], %[t0]\n\t" \
  "s_addc_u32 s91, %[wh], 0\n\t" \
  "s_add_u32 m0, s95, %[mb]\n\t" \
  "s_add_u32 s92, s92, 1\n\t" \
  "s_and_b32 s92, s92, 15\n\t" \
  "global_load_lds_dwordx4 %[av0], s[90:91]\n\t" \
  "global_load_lds_dwordx4 %[av1], s[90:91] offset:1024\n\t" \
  "s_add_u32 s95, s95, 0x4000\n\t" \
  "s_and_b32 s95, s95, 0xc000\n\t" \
  "s_lshl_b32 %[t0], s93, 13\n\t" \
  "s_add_u32 s96, %[zl], %[t0]\n\t" \
  "s_addc_u32 s97, %[zh], 0\n\t" \
  "s_add_u32 s93, s93, 1\n\t" \
  "s_and_b32 s93, s93, 15\n\t" \
  "s_waitcnt lgkmcnt(6)\n\t" \
  "v_mfma_f32_32x32x16_bf16 %[g1], v[80:83], v[100:103], %[g1]\n\t" \
  "ds_read_b128 v[96:99], %[a0] offset:32768\n\t" \
  "ds_read_b128 v[100:103], %[c0] offset:32768\n\t" \
  "global_load_dwordx4 v[80:83], %[l16], s[96:97] offset:0 sc1\n\t" \
  "s_waitcnt vmcnt(11) lgkmcnt(7)\n\t" \
  "v_mfma_f32_32x32x16_bf16 %[g0], v[84:87], v[104:107], %[g0]\n\t" \
  "s_waitcnt lgkmcnt(6)\n\t" \
  "v_mfma_f32_32x32x16_bf16 %[g1], v[84:87], v[108:111], %[g1]\n\t" \
  "ds_read_b128 v[104:107], %[a1] offset:32768\n\t" \
  "ds_read_b128 v[108:111], %[c1] offset:32768\n\t" \
  "global_load_dwordx4 v[84:87], %[l16], s[96:97] offset:1024 sc1\n\t" \
  "s_waitcnt vmcnt(11) lgkmcnt(7)\n\t" \
  "v_mfma_f32_32x32x16_bf16 %[g0], v[88:91], v[112:115], %[g0]\n\t" \
  "s_waitcnt lgkmcnt(6)\n\t" \
  "v_mfma_f32_32x32x16_bf16 %[g1], v[88:91], v[116:119], %[g1]\n\t" \
  "ds_read_b128 v[112:115], %[a2] offset:32768\n\t" \
  "ds_read_b128 v[116:119], %[c2] offset:32768\n\t" \
  "global_load_dwordx4 v[88:91], %[l16], s[96:97] offset:2048 sc1\n\t" \
  "s_waitcnt vmcnt(11) lgkmcnt(7)\n\t" \
  "v_mfma_f32_32x32x16_bf16 %[g0], v[92:95], v[120:123], %[g0]\n\t" \
  "s_waitcnt lgkmcnt(6)\n\t" \
  "v_mfma_f32_32x32x16_bf16 %[g1], v[92:95], v[124:127], %[g1]\n\t" \
  "ds_read_b128 v[120:123], %[a3] offset:32768\n\t" \
  "ds_read_b128 v[124:127], %[c3] offset:32768\n\t" \
  "global_load_dwordx4 v[92:95], %[l16], s[96:97] offset:3072 sc1\n\t" \
  "s_waitcnt vmcnt(10)\n\t" \
  "s_barrier\n\t" \
  "s_waitcnt vmcnt(9) lgkmcnt(7)\n\t" \
  "v_mfma_f32_32x32x16_bf16 %[g0], v[64:67], v[96:99], %[g0]\n\t" \
  "s_lshl_b32 %[t0], s92, 8\n\t" \
  "s_add_u32 s90, %[wl], %[t0]\n\t" \
  "s_addc_u32 s91, %[wh], 0\n\t" \
  "s_add_u32 m0, s95, %[mb]\n\t" \
  "s_add_u32 s92, s92, 1\n\t" \
  "s_and_b32 s92, s92, 15\n\t" \
  "global_load_lds_dwordx4 %[av0], s[90:91]\n\t" \
  "global_load_lds_dwordx4 %[av1], s[90:91] offset:1024\n\t" \
  "s_add_u32 s95, s95, 0x4000\n\t" \
  "s_and_b32 s95, s95, 0xc000\n\t" \
  "s_lshl_b32 %[t0], s93, 13\n\t" \
  "s_add_u32 s96, %[zl], %[t0]\n\t" \
  "s_addc_u32 s97, %[zh], 0\n\t" \
  "s_add_u32 s93, s93, 1\n\t" \
  "s_and_b32 s93, s93, 15\n\t" \
  "s_waitcnt lgkmcnt(6)\n\t" \
  "v_mfma_f32_32x32x16_bf16 %[g1], v[64:67], v[100:103], %[g1]\n\t" \
  "ds_read_b128 v[96:99], %[a0] offset:49152\n\t" \
  "ds_read_b128 v[100:103], %[c0] offset:49152\n\t" \
  "global_load_dwordx4 v[64:67], %[l16], s[96:97] offset:0 sc1\n\t" \
  "s_waitcnt vmcnt(11) lgkmcnt(7)\n\t" \
  "v_mfma_f32_32x32x16_bf16 %[g0], v[68:71], v[104:107], %[g0]\n\t" \
  "s_waitcnt lgkmcnt(6)\n\t" \
  "v_mfma_f32_32x32x16_bf16 %[g1], v[68:71], v[108:111], %[g1]\n\t" \
  "ds_read_b128 v[104:107], %[a1] offset:49152\n\t" \
  "ds_read_b128 v[108:111], %[c1] offset:49152\n\t" \
  "global_load_dwordx4 v[68:71], %[l16], s[96:97] offset:1024 sc1\n\t" \
  "s_waitcnt vmcnt(11) lgkmcnt(7)\n\t" \
  "v_mfma_f32_32x32x16_bf16 %[g0], v[72:75], v[112:115], %[g0]\n\t" \
  "s_waitcnt lgkmcnt(6)\n\t" \
  "v_mfma_f32_32x32x16_bf16 %[g1], v[72:75], v[116:119], %[g1]\n\t" \
  "ds_read_b128 v[112:115], %[a2] offset:49152\n\t" \
  "ds_read_b128 v[116:119], %[c2] offset:49152\n\t" \
  "global_load_dwordx4 v[72:75], %[l16], s[96:97] offset:2048 sc1\n\t" \
  "s_waitcnt vmcnt(11) lgkmcnt(7)\n\t" \
  "v_mfma_f32_32x32x16_bf16 %[g0], v[76:79], v[120:123], %[g0]\n\t" \
  "s_waitcnt lgkmcnt(6)\n\t" \
  "v_mfma_f32_32x32x16_bf16 %[g1], v[76:79], v[124:127], %[g1]\n\t" \
  "ds_read_b128 v[120:123], %[a3] offset:49152\n\t" \
  "ds_read_b128 v[124:127], %[c3] offset:49152\n\t" \
  "global_load_dwordx4 v[76:79], %[l16], s[96:97] offset:3072 sc1\n\t" \
  "s_waitcnt vmcnt(10)\n\t" \
  "s_barrier\n\t" \
  "s_waitcnt vmcnt(9) lgkmcnt(7)\n\t" \
  "v_mfma_f32_32x32x16_bf16 %[g0], v[80:83], v[96:99], %[g0]\n\t" \
  "s_lshl_b32 %[t0], s92, 8\n\t" \
  "s_add_u32 s90, %[wl], %[t0]\n\t" \
  "s_addc_u32 s91, %[wh], 0\n\t" \
  "s_add_u32 m0, s95, %[mb]\n\t" \
  "s_add_u32 s92, s92, 1\n\t" \
  "s_and_b32 s92, s92, 15\n\t" \
  "global_load_lds_dwordx4 %[av0], s[90:91]\n\t" \
  "global_load_lds_dwordx4 %[av1], s[90:91] offset:1024\n\t" \
  "s_add_u32 s95, s95, 0x4000\n\t" \
  "s_and_b32 s95, s95, 0xc000\n\t" \
  "s_lshl_b32 %[t0], s93, 13\n\t" \
  "s_add_u32 s96, %[zl], %[t0]\n\t" \
  "s_addc_u32 s97, %[zh], 0\n\t" \
  "s_add_u32 s93, s93, 1\n\t" \
  "s_and_b32 s93, s93, 15\n\t" \
  "s_waitcnt lgkmcnt(6)\n\t" \
  "v_mfma_f32_32x32x16_bf16 %[g1], v[80:83], v[100:103], %[g1]\n\t" \
  "ds_read_b128 v[96:99], %[a0] offset:0\n\t" \
  "ds_read_b128 v[100:103], %[c0] offset:0\n\t" \
  "global_load_dwordx4 v[80:83], %[l16], s[96:97] offset:0 sc1\n\t" \
  "s_waitcnt vmcnt(11) lgkmcnt(7)\n\t" \
  "v_mfma_f32_32x32x16_bf16 %[g0], v[84:87], v[104:107], %[g0]\n\t" \
  "s_waitcnt lgkmcnt(6)\n\t" \
  "v_mfma_f32_32x32x16_bf16 %[g1], v[84:87], v[108:111], %[g1]\n\t" \
  "ds_read_b128 v[104:107], %[a1] offset:0\n\t" \
  "ds_read_b128 v[108:111], %[c1] offset:0\n\t" \
  "global_load_dwordx4 v[84:87], %[l16], s[96:97] offset:1024 sc1\n\t" \
  "s_waitcnt vmcnt(11) lgkmcnt(7)\n\t" \
  "v_mfma_f32_32x32x16_bf16 %[g0], v[88:91], v[112:115], %[g0]\n\t" \
  "s_waitcnt lgkmcnt(6)\n\t" \
  "v_mfma_f32_32x32x16_bf16 %[g1], v[88:91], v[116:119], %[g1]\n\t" \
  "ds_read_b128 v[112:115], %[a2] offset:0\n\t" \
  "ds_read_b128 v[116:119], %[c2] offset:0\n\t" \
  "global_load_dwordx4 v[88:91], %[l16], s[96:97] offset:2048 sc1\n\t" \
  "s_waitcnt vmcnt(11) lgkmcnt(7)\n\t" \
  "v_mfma_f32_32x32x16_bf16 %[g0], v[92:95], v[120:123], %[g0]\n\t" \
  "s_waitcnt lgkmcnt(6)\n\t" \
  "v_mfma_f32_32x32x16_bf16 %[g1], v[92:95], v[124:127], %[g1]\n\t" \
  "ds_read_b128 v[120:123], %[a3] offset:0\n\t" \
  "ds_read_b128 v[124:127], %[c3] offset:0\n\t" \
  "global_load_dwordx4 v[92:95], %[l16], s[96:97] offset:3072 sc1\n\t" \
  "s_waitcnt vmcnt(10)\n\t" \
  "s_barrier\n\t" \
  "s_sub_u32 s98, s98, 1\n\t" \
  "s_cmp_lg_u32 s98, 0\n\t" \
  "s_cbranch_scc1 .Ls%=\n\t" \
  "s_waitcnt vmcnt(9) lgkmcnt(7)\n\t" \
  "v_mfma_f32_32x32x16_bf16 %[g0], v[64:67], v[96:99], %[g0]\n\t" \
  "s_lshl_b32 %[t0], s92, 8\n\t" \
  "s_add_u32 s90, %[wl], %[t0]\n\t" \
  "s_addc_u32 s91, %[wh], 0\n\t" \
  "s_add_u32 m0, s95, %[mb]\n\t" \
  "s_add_u32 s92, s92, 1\n\t" \
  "s_and_b32 s92, s92, 15\n\t" \
  "global_load_lds_dwordx4 %[av0], s[90:91]\n\t" \
  "global_load_lds_dwordx4 %[av1], s[90:91] offset:1024\n\t" \
  "s_add_u32 s95, s95, 0x4000\n\t" \
  "s_and_b32 s95, s95, 0xc000\n\t" \
  "s_lshl_b32 %[t0], s93, 13\n\t" \
  "s_add_u32 s96, %[zl], %[t0]\n\t" \
  "s_addc_u32 s97, %[zh], 0\n\t" \
  "s_add_u32 s93, s93, 1\n\t" \
  "s_and_b32 s93, s93, 15\n\t" \
  "s_waitcnt lgkmcnt(6)\n\t" \
  "v_mfma_f32_32x32x16_bf16 %[g1], v[64:67], v[100:103], %[g1]\n\t" \
  "ds_read_b128 v[96:99], %[a0] offset:16384\n\t" \
  "ds_read_b128 v[100:103], %[c0] offset:16384\n\t" \
  "global_load_dwordx4 v[64:67], %[l16], s[96:97] offset:0 sc1\n\t" \
  "s_waitcnt vmcnt(11) lgkmcnt(7)\n\t" \
  "v_mfma_f32_32x32x16_bf16 %[g0], v[68:71], v[104:107], %[g0]\n\t" \
  "s_waitcnt lgkmcnt(6)\n\t" \
  "v_mfma_f32_32x32x16_bf16 %[g1], v[68:71], v[108:111], %[g1]\n\t" \
  "ds_read_b128 v[104:107], %[a1] offset:16384\n\t" \
  "ds_read_b128 v[108:111], %[c1] offset:16384\n\t" \
  "global_load_dwordx4 v[68:71], %[l16], s[96:97] offset:1024 sc1\n\t" \
  "s_waitcnt vmcnt(11) lgkmcnt(7)\n\t" \
  "v_mfma_f32_32x32x16_bf16 %[g0], v[72:75], v[112:115], %[g0]\n\t" \
  "s_waitcnt lgkmcnt(6)\n\t" \
  "v_mfma_f32_32x32x16_bf16 %[g1], v[72:75], v[116:119], %[g1]\n\t" \
  "ds_read_b128 v[112:115], %[a2] offset:16384\n\t" \
  "ds_read_b128 v[116:119], %[c2] offset:16384\n\t" \
  "global_load_dwordx4 v[72:75], %[l16], s[96:97] offset:2048 sc1\n\t" \
  "s_waitcnt vmcnt(11) lgkmcnt(7)\n\t" \
  "v_mfma_f32_32x32x16_bf16 %[g0], v[76:79], v[120:123], %[g0]\n\t" \
  "s_waitcnt lgkmcnt(6)\n\t" \
  "v_mfma_f32_32x32x16_bf16 %[g1], v[76:79], v[124:127], %[g1]\n\t" \
  "ds_read_b128 v[120:123], %[a3] offset:16384\n\t" \
  "ds_read_b128 v[124:127], %[c3] offset:16384\n\t" \
  "global_load_dwordx4 v[76:79], %[l16], s[96:97] offset:3072 sc1\n\t" \
  "s_waitcnt vmcnt(10)\n\t" \
  "s_barrier\n\t" \
  "s_waitcnt vmcnt(9) lgkmcnt(7)\n\t" \
  "v_mfma_f32_32x32x16_bf16 %[g0], v[80:83], v[96:99], %[g0]\n\t" \
  "s_add_u32 m0, %[mb], 2048\n\t" \
  "s_nop 0\n\t" \
  "global_load_lds_dwordx4 %[l16], %[wf] offset:-2048\n\t" \
  "global_load_lds_dwordx4 %[l16], %[wf] offset:-1024\n\t" \
  "s_lshl_b32 %[t0], s93, 13\n\t" \
  "s_add_u32 s96, %[zl], %[t0]\n\t" \
  "s_addc_u32 s97, %[zh], 0\n\t" \
  "s_add_u32 s93, s93, 1\n\t" \
  "s_and_b32 s93, s93, 15\n\t" \
  "s_waitcnt lgkmcnt(6)\n\t" \
  "v_mfma_f32_32x32x16_bf16 %[g1], v[80:83], v[100:103], %[g1]\n\t" \
  "ds_read_b128 v[96:99], %[a0] offset:32768\n\t" \
  "ds_read_b128 v[100:103], %[c0] offset:32768\n\t" \
  "global_load_dwordx4 v[80:83], %[l16], s[96:97] offset:0 sc1\n\t" \
  "s_waitcnt vmcnt(11) lgkmcnt(7)\n\t" \
  "v_mfma_f32_32x32x16_bf16 %[g0], v[84:87], v[104:107], %[g0]\n\t" \
  "s_waitcnt lgkmcnt(6)\n\t" \
  "v_mfma_f32_32x32x16_bf16 %[g1], v[84:87], v[108:111], %[g1]\n\t" \
  "ds_read_b128 v[104:107], %[a1] offset:32768\n\t" \
  "ds_read_b128 v[108:111], %[c1] offset:32768\n\t" \
  "global_load_dwordx4 v[84:87], %[l16], s[96:97] offset:1024 sc1\n\t" \
  "s_waitcnt vmcnt(11) lgkmcnt(7)\n\t" \
  "v_mfma_f32_32x32x16_bf16 %[g0], v[88:91], v[112:115], %[g0]\n\t" \
  "s_waitcnt lgkmcnt(6)\n\t" \
  "v_mfma_f32_32x32x16_bf16 %[g1], v[88:91], v[116:119], %[g1]\n\t" \
  "ds_read_b128 v[112:115], %[a2] offset:32768\n\t" \
  "ds_read_b128 v[116:119], %[c2] offset:32768\n\t" \
  "global_load_dwordx4 v[88:91], %[l16], s[96:97] offset:2048 sc1\n\t" \
  "s_waitcnt vmcnt(11) lgkmcnt(7)\n\t" \
  "v_mfma_f32_32x32x16_bf16 %[g0], v[92:95], v[120:123], %[g0]\n\t" \
  "s_waitcnt lgkmcnt(6)\n\t" \
  "v_mfma_f32_32x32x16_bf16 %[g1], v[92:95], v[124:127], %[g1]\n\t" \
  "ds_read_b128 v[120:123], %[a3] offset:32768\n\t" \
  "ds_read_b128 v[124:127], %[c3] offset:32768\n\t" \
  "global_load_dwordx4 v[92:95], %[l16], s[96:97] offset:3072 sc1\n\t" \
  "s_waitcnt vmcnt(10)\n\t" \
  "s_barrier\n\t" \
  "s_waitcnt vmcnt(9) lgkmcnt(7)\n\t" \
  "v_mfma_f32_32x32x16_bf16 %[g0], v[64:67], v[96:99], %[g0]\n\t" \
  "s_add_u32 m0, %[mb], 16384\n\t" \
  "s_nop 0\n\t" \
  "global_load_lds_dwordx4 %[l16], %[wf] offset:0\n\t" \
  "global_load_lds_dwordx4 %[l16], %[wf] offset:1024\n\t" \
  "v_bfe_u32 %[bb], %[l16], 4, 5\n\t" \
  "v_lshrrev_b32 %[pf], 9, %[l16]\n\t" \
  "v_lshlrev_b32 %[pf], 3, %[pf]\n\t" \
  "v_lshl_add_u32 %[pf], %[bb], 10, %[pf]\n\t" \
  "v_lshlrev_b32 %[bb], 1, %[bb]\n\t" \
  "global_load_dwordx2 %[x0], %[pf], %[xp]\n\t" \
  "global_load_dwordx2 %[x1], %[pf], %[xp] offset:16\n\t" \
  "global_load_ushort %[bb], %[bb], %[bp]\n\t" \
  "v_mov_b32 %[pf], 0\n\t" \
  "global_load_dword %[pf], %[pf], %[up] sc1\n\t" \
  "s_waitcnt lgkmcnt(6)\n\t" \
  "v_mfma_f32_32x32x16_bf16 %[g1], v[64:67], v[100:103], %[g1]\n\t" \
  "ds_read_b128 v[96:99], %[a0] offset:49152\n\t" \
  "ds_read_b128 v[100:103], %[c0] offset:49152\n\t" \
  "s_waitcnt vmcnt(14) lgkmcnt(7)\n\t" \
  "v_mfma_f32_32x32x16_bf16 %[g0], v[68:71], v[104:107], %[g0]\n\t" \
  "s_waitcnt lgkmcnt(6)\n\t" \
  "v_mfma_f32_32x32x16_bf16 %[g1], v[68:71], v[108:111], %[g1]\n\t" \
  "ds_read_b128 v[104:107], %[a1] offset:49152\n\t" \
  "ds_read_b128 v[108:111], %[c1] offset:49152\n\t" \
  "s_waitcnt vmcnt(13) lgkmcnt(7)\n\t" \
  "v_mfma_f32_32x32x16_bf16 %[g0], v[72:75], v[112:115], %[g0]\n\t" \
  "s_waitcnt lgkmcnt(6)\n\t" \
  "v_mfma_f32_32x32x16_bf16 %[g1], v[72:75], v[116:119], %[g1]\n\t" \
  "ds_read_b128 v[112:115], %[a2] offset:49152\n\t" \
  "ds_read_b128 v[116:119], %[c2] offset:49152\n\t" \
  "s_waitcnt vmcnt(12) lgkmcnt(7)\n\t" \
  "v_mfma_f32_32x32x16_bf16 %[g0], v[76:79], v[120:123], %[g0]\n\t" \
  "s_waitcnt lgkmcnt(6)\n\t" \
  "v_mfma_f32_32x32x16_bf16 %[g1], v[76:79], v[124:127], %[g1]\n\t" \
  "ds_read_b128 v[120:123], %[a3] offset:49152\n\t" \
  "ds_read_b128 v[124:127], %[c3] offset:49152\n\t" \
  "s_barrier\n\t" \
  "s_waitcnt vmcnt(9) lgkmcnt(7)\n\t" \
  "v_mfma_f32_32x32x16_bf16 %[g0], v[80:83], v[96:99], %[g0]\n\t" \
  "s_add_u32 m0, %[mb], 30720\n\t" \
  "s_nop 0\n\t" \
  "global_load_lds_dwordx4 %[l16], %[wf] offset:2048\n\t" \
  "global_load_lds_dwordx4 %[l16], %[wf] offset:3072\n\t" \
  "s_waitcnt lgkmcnt(6)\n\t" \
  "v_mfma_f32_32x32x16_bf16 %[g1], v[80:83], v[100:103], %[g1]\n\t" \
  "s_waitcnt vmcnt(10) lgkmcnt(5)\n\t" \
  "v_mfma_f32_32x32x16_bf16 %[g0], v[84:87], v[104:107], %[g0]\n\t" \
  "s_waitcnt lgkmcnt(4)\n\t" \
  "v_mfma_f32_32x32x16_bf16 %[g1], v[84:87], v[108:111], %[g1]\n\t" \
  "s_waitcnt vmcnt(9) lgkmcnt(3)\n\t" \
  "v_mfma_f32_32x32x16_bf16 %[g0], v[88:91], v[112:115], %[g0]\n\t" \
  "s_waitcnt lgkmcnt(2)\n\t" \
  "v_mfma_f32_32x32x16_bf16 %[g1], v[88:91], v[116:119], %[g1]\n\t" \
  "s_waitcnt vmcnt(8) lgkmcnt(1)\n\t" \
  "v_mfma_f32_32x32x16_bf16 %[g0], v[92:95], v[120:123], %[g0]\n\t" \
  "s_waitcnt lgkmcnt(0)\n\t" \
  "v_mfma_f32_32x32x16_bf16 %[g1], v[92:95], v[124:127], %[g1]\n\t" \
  "s_barrier\n\t" \
  "s_waitcnt vmcnt(0)\n\t" \
  "s_nop 15\n\t" \
  ""
#define G_F_ASM \
  "s_nop 4\n\t" \
  "s_add_u32 m0, %[mb], 2048\n\t" \
  "s_nop 0\n\t" \
  "global_load_lds_dwordx4 %[l16], %[wf] offset:-2048\n\t" \
  "global_load_lds_dwordx4 %[l16], %[wf] offset:-1024\n\t" \
  "s_add_u32 m0, %[mb], 16384\n\t" \
  "s_nop 0\n\t" \
  "global_load_lds_dwordx4 %[l16], %[wf] offset:0\n\t" \
  "global_load_lds_dwordx4 %[l16], %[wf] offset:1024\n\t" \
  "s_add_u32 m0, %[mb], 30720\n\t" \
  "s_nop 0\n\t" \
  "global_load_lds_dwordx4 %[l16], %[wf] offset:2048\n\t" \
  "global_load_lds_dwordx4 %[l16], %[wf] offset:3072\n\t" \
  "s_waitcnt vmcnt(0)\n\t" \
  "s_barrier\n\t" \
  ""
#define G_U_ASM \
  "s_nop 4\n\t" \
  "s_mov_b64 s[94:95], %[ap]\n\t" \
  "s_mov_b32 s98, 0x40000\n\t" \
  "v_add_u32 %[b0], %[sb0], %[lb]\n\t" \
  "v_xor_b32 %[b1], 0x2000, %[b0]\n\t" \
  "ds_read_b128 v[48:51], %[b0] offset:0\n\t" \
  "ds_read_b128 v[52:55], %[b1] offset:0\n\t" \
  "ds_read_b128 v[56:59], %[b0] offset:1024\n\t" \
  "ds_read_b128 v[60:63], %[b1] offset:1024\n\t" \
  "ds_read_b128 v[64:67], %[b0] offset:16384\n\t" \
  "ds_read_b128 v[68:71], %[b1] offset:16384\n\t" \
  "ds_read_b128 v[72:75], %[b0] offset:17408\n\t" \
  "ds_read_b128 v[76:79], %[b1] offset:17408\n\t" \
  "ds_read_b128 v[80:83], %[b0] offset:32768\n\t" \
  "ds_read_b128 v[84:87], %[b1] offset:32768\n\t" \
  "ds_read_b128 v[88:91], %[b0] offset:33792\n\t" \
  "ds_read_b128 v[92:95], %[b1] offset:33792\n\t" \
  "v_mov_b32 v96, 1\n\t" \
  "v_mov_b32 v97, 0\n\t" \
  "s_waitcnt lgkmcnt(0)\n\t" \
  "s_barrier\n\t" \
  "s_cmp_lg_u32 %[wid], 0\n\t" \
  "s_cbranch_scc1 .Lnh%=\n\t" \
  "s_mov_b64 exec, 1\n\t" \
  "global_store_dword v97, v96, %[uf] sc1\n\t" \
  "s_mov_b64 exec, -1\n\t" \
  ".Lnh%=:\n\t" \
  "s_add_u32 m0, %[mb], 2048\n\t" \
  "s_nop 0\n\t" \
  "global_load_lds_dwordx4 %[l16], %[w2] offset:-2048\n\t" \
  "global_load_lds_dwordx4 %[l16], %[w2] offset:-1024\n\t" \
  "s_add_u32 m0, %[mb], 16384\n\t" \
  "s_nop 0\n\t" \
  "global_load_lds_dwordx4 %[l16], %[w2] offset:0\n\t" \
  "global_load_lds_dwordx4 %[l16], %[w2] offset:1024\n\t" \
  "s_add_u32 m0, %[mb], 30720\n\t" \
  "s_nop 0\n\t" \
  "global_load_lds_dwordx4 %[l16], %[w2] offset:2048\n\t" \
  "global_load_lds_dwordx4 %[l16], %[w2] offset:3072\n\t" \
  "s_mov_b32 m0, %[m4]\n\t" \
  "s_nop 0\n\t" \
  "global_load_lds_dwordx4 %[l16], %[wg]\n\t" \
  "global_load_lds_dwordx4 %[l16], %[wg] offset:1024\n\t" \
  "global_load_lds_dwordx4 %[l16], %[wh] offset:2048\n\t" \
  "global_load_lds_dwordx4 %[l16], %[wh] offset:3072\n\t" \
  "s_cmp_lg_u32 %[wid], 0\n\t" \
  "s_cbranch_scc1 .Lqu%=\n\t" \
  "v_lshrrev_b32 v102, 2, %[l16]\n\t" \
  "v_and_b32 v102, 28, v102\n\t" \
  "s_mov_b32 %[t0], 0x40000\n\t" \
  ".Lpu%=:\n\t" \
  "global_load_dword v100, v102, %[fu] sc1\n\t" \
  "s_waitcnt vmcnt(0)\n\t" \
  "v_cmp_eq_u32 vcc, 0, v100\n\t" \
  "s_cbranch_vccz .Lqu%=\n\t" \
  "s_sleep 4\n\t" \
  "s_sub_u32 %[t0], %[t0], 1\n\t" \
  "s_cbranch_scc0 .Lpu%=\n\t" \
  ".Lqu%=:\n\t" \
  "s_barrier\n\t" \
  "s_setprio 0\n\t" \
  "global_load_dwordx4 v[96:99], %[l16], s[94:95] offset:-4096 sc1\n\t" \
  "global_load_dwordx4 v[100:103], %[l16], s[94:95] offset:-3072 sc1\n\t" \
  "global_load_dwordx4 v[104:107], %[l16], s[94:95] offset:-2048 sc1\n\t" \
  "global_load_dwordx4 v[108:111], %[l16], s[94:95] offset:-1024 sc1\n\t" \
  "global_load_dwordx4 v[112:115], %[l16], s[94:95] offset:0 sc1\n\t" \
  "global_load_dwordx4 v[116:119], %[l16], s[94:95] offset:1024 sc1\n\t" \
  "global_load_dwordx4 v[120:123], %[l16], s[94:95] offset:2048 sc1\n\t" \
  "global_load_dwordx4 v[124:127], %[l16], s[94:95] offset:3072 sc1\n\t" \
  "s_mov_b64 s[90:91], %[o0]\n\t" \
  "s_mov_b64 s[92:93], %[o1]\n\t" \
  "s_waitcnt vmcnt(7)\n\t" \
  "s_barrier\n\t" \
  "v_max3_u32 %[tv], v96, v97, v98\n\t" \
  "v_max_u32 %[tv], %[tv], v99\n\t" \
  "v_cmp_eq_u32 vcc, -1, %[tv]\n\t" \
  "s_cbranch_vccnz .Lru0_%=\n\t" \
  ".Lbu0_%=:\n\t" \
  "v_mfma_f32_32x32x16_bf16 %[c0], v[48:51], v[96:99], %[c0]\n\t" \
  "v_mfma_f32_32x32x16_bf16 %[c1], v[52:55], v[96:99], 0\n\t" \
  "s_add_u32 s94, s94, 0x2000\n\t" \
  "s_addc_u32 s95, s95, 0\n\t" \
  "global_load_dwordx4 v[96:99], %[l16], s[94:95] offset:-4096 sc1\n\t" \
  "ds_read_b128 v[48:51], %[b0] offset:0\n\t" \
  "ds_read_b128 v[52:55], %[b1] offset:0\n\t" \
  "s_waitcnt vmcnt(7)\n\t" \
  "v_max3_u32 %[tv], v100, v101, v102\n\t" \
  "v_max_u32 %[tv], %[tv], v103\n\t" \
  "v_cmp_eq_u32 vcc, -1, %[tv]\n\t" \
  "s_cbranch_vccnz .Lru1_%=\n\t" \
  ".Lbu1_%=:\n\t" \
  "v_mfma_f32_32x32x16_bf16 %[c0], v[56:59], v[100:103], %[c0]\n\t" \
  "v_mfma_f32_32x32x16_bf16 %[c1], v[60:63], v[100:103], %[c1]\n\t" \
  "global_load_dwordx4 v[100:103], %[l16], s[94:95] offset:-3072 sc1\n\t" \
  "ds_read_b128 v[56:59], %[b0] offset:1024\n\t" \
  "ds_read_b128 v[60:63], %[b1] offset:1024\n\t" \
  "s_waitcnt vmcnt(7)\n\t" \
  "v_max3_u32 %[tv], v104, v105, v106\n\t" \
  "v_max_u32 %[tv], %[tv], v107\n\t" \
  "v_cmp_eq_u32 vcc, -1, %[tv]\n\t" \
  "s_cbranch_vccnz .Lru2_%=\n\t" \
  ".Lbu2_%=:\n\t" \
  "v_mfma_f32_32x32x16_bf16 %[c0], v[64:67], v[104:107], %[c0]\n\t" \
  "v_mfma_f32_32x32x16_bf16 %[c1], v[68:71], v[104:107], %[c1]\n\t" \
  "global_load_dwordx4 v[104:107], %[l16], s[94:95] offset:-2048 sc1\n\t" \
  "ds_read_b128 v[64:67], %[b0] offset:16384\n\t" \
  "ds_read_b128 v[68:71], %[b1] offset:16384\n\t" \
  "s_waitcnt vmcnt(7)\n\t" \
  "v_max3_u32 %[tv], v108, v109, v110\n\t" \
  "v_max_u32 %[tv], %[tv], v111\n\t" \
  "v_cmp_eq_u32 vcc, -1, %[tv]\n\t" \
  "s_cbranch_vccnz .Lru3_%=\n\t" \
  ".Lbu3_%=:\n\t" \
  "v_mfma_f32_32x32x16_bf16 %[c0], v[72:75], v[108:111], %[c0]\n\t" \
  "v_mfma_f32_32x32x16_bf16 %[c1], v[76:79], v[108:111], %[c1]\n\t" \
  "global_load_dwordx4 v[108:111], %[l16], s[94:95] offset:-1024 sc1\n\t" \
  "ds_read_b128 v[72:75], %[b0] offset:17408\n\t" \
  "ds_read_b128 v[76:79], %[b1] offset:17408\n\t" \
  "s_waitcnt vmcnt(7)\n\t" \
  "v_max3_u32 %[tv], v112, v113, v114\n\t" \
  "v_max_u32 %[tv], %[tv], v115\n\t" \
  "v_cmp_eq_u32 vcc, -1, %[tv]\n\t" \
  "s_cbranch_vccnz .Lru4_%=\n\t" \
  ".Lbu4_%=:\n\t" \
  "v_mfma_f32_32x32x16_bf16 %[c0], v[80:83], v[112:115], %[c0]\n\t" \
  "v_mfma_f32_32x32x16_bf16 %[c1], v[84:87], v[112:115], %[c1]\n\t" \
  "global_load_dwordx4 v[112:115], %[l16], s[94:95] offset:0 sc1\n\t" \
  "ds_read_b128 v[80:83], %[b0] offset:32768\n\t" \
  "ds_read_b128 v[84:87], %[b1] offset:32768\n\t" \
  "s_waitcnt vmcnt(7)\n\t" \
  "v_max3_u32 %[tv], v116, v117, v118\n\t" \
  "v_max_u32 %[tv], %[tv], v119\n\t" \
  "v_cmp_eq_u32 vcc, -1, %[tv]\n\t" \
  "s_cbranch_vccnz .Lru5_%=\n\t" \
  ".Lbu5_%=:\n\t" \
  "v_mfma_f32_32x32x16_bf16 %[c0], v[88:91], v[116:119], %[c0]\n\t" \
  "v_mfma_f32_32x32x16_bf16 %[c1], v[92:95], v[116:119], %[c1]\n\t" \
  "global_load_dwordx4 v[116:119], %[l16], s[94:95] offset:1024 sc1\n\t" \
  "ds_read_b128 v[88:91], %[b0] offset:33792\n\t" \
  "ds_read_b128 v[92:95], %[b1] offset:33792\n\t" \
  "s_waitcnt vmcnt(7)\n\t" \
  "v_max3_u32 %[tv], v120, v121, v122\n\t" \
  "v_max_u32 %[tv], %[tv], v123\n\t" \
  "v_cmp_eq_u32 vcc, -1, %[tv]\n\t" \
  "s_cbranch_vccnz .Lru6_%=\n\t" \
  ".Lbu6_%=:\n\t" \
  "s_waitcnt lgkmcnt(10)\n\t" \
  "v_mfma_f32_32x32x16_bf16 %[c0], v[48:51], v[120:123], %[c0]\n\t" \
  "v_mfma_f32_32x32x16_bf16 %[c1], v[52:55], v[120:123], %[c1]\n\t" \
  "v_add_u32 %[b0], %[se], %[lb]\n\t" \
  "v_xor_b32 %[b1], 0x1c000, %[b0]\n\t" \
  "ds_read_b128 v[48:51], %[b0] offset:0\n\t" \
  "ds_read_b128 v[52:55], %[b1] offset:0\n\t" \
  "s_waitcnt vmcnt(6)\n\t" \
  "v_max3_u32 %[tv], v124, v125, v126\n\t" \
  "v_max_u32 %[tv], %[tv], v127\n\t" \
  "v_cmp_eq_u32 vcc, -1, %[tv]\n\t" \
  "s_cbranch_vccnz .Lru7_%=\n\t" \
  ".Lbu7_%=:\n\t" \
  "s_waitcnt lgkmcnt(10)\n\t" \
  "v_mfma_f32_32x32x16_bf16 %[c0], v[56:59], v[124:127], %[c0]\n\t" \
  "v_mfma_f32_32x32x16_bf16 %[c1], v[60:63], v[124:127], %[c1]\n\t" \
  "ds_read_b128 v[56:59], %[b0] offset:1024\n\t" \
  "ds_read_b128 v[60:63], %[b1] offset:1024\n\t" \
  "s_waitcnt vmcnt(5)\n\t" \
  "v_max3_u32 %[tv], v96, v97, v98\n\t" \
  "v_max_u32 %[tv], %[tv], v99\n\t" \
  "v_cmp_eq_u32 vcc, -1, %[tv]\n\t" \
  "s_cbranch_vccnz .Lru8_%=\n\t" \
  ".Lbu8_%=:\n\t" \
  "s_waitcnt lgkmcnt(10)\n\t" \
  "v_mfma_f32_32x32x16_bf16 %[c0], v[64:67], v[96:99], %[c0]\n\t" \
  "v_mfma_f32_32x32x16_bf16 %[c1], v[68:71], v[96:99], %[c1]\n\t" \
  "global_load_dwordx4 v[64:67], %[lb], s[90:91] offset:-4096\n\t" \
  "global_load_dwordx4 v[68:71], %[lb], s[92:93] offset:-4096\n\t" \
  "s_waitcnt vmcnt(6)\n\t" \
  "v_max3_u32 %[tv], v100, v101, v102\n\t" \
  "v_max_u32 %[tv], %[tv], v103\n\t" \
  "v_cmp_eq_u32 vcc, -1, %[tv]\n\t" \
  "s_cbranch_vccnz .Lru9_%=\n\t" \
  ".Lbu9_%=:\n\t" \
  "s_waitcnt lgkmcnt(8)\n\t" \
  "v_mfma_f32_32x32x16_bf16 %[c0], v[72:75], v[100:103], %[c0]\n\t" \
  "v_mfma_f32_32x32x16_bf16 %[c1], v[76:79], v[100:103], %[c1]\n\t" \
  "global_load_dwordx4 v[72:75], %[lb], s[90:91] offset:-3072\n\t" \
  "global_load_dwordx4 v[76:79], %[lb], s[92:93] offset:-3072\n\t" \
  "s_waitcnt vmcnt(7)\n\t" \
  "v_max3_u32 %[tv], v104, v105, v106\n\t" \
  "v_max_u32 %[tv], %[tv], v107\n\t" \
  "v_cmp_eq_u32 vcc, -1, %[tv]\n\t" \
  "s_cbranch_vccnz .Lru10_%=\n\t" \
  ".Lbu10_%=:\n\t" \
  "s_waitcnt lgkmcnt(6)\n\t" \
  "v_mfma_f32_32x32x16_bf16 %[c0], v[80:83], v[104:107], %[c0]\n\t" \
  "v_mfma_f32_32x32x16_bf16 %[c1], v[84:87], v[104:107], %[c1]\n\t" \
  "global_load_dwordx4 v[80:83], %[lb], s[90:91] offset:-2048\n\t" \
  "global_load_dwordx4 v[84:87], %[lb], s[92:93] offset:-2048\n\t" \
  "s_waitcnt vmcnt(8)\n\t" \
  "v_max3_u32 %[tv], v108, v109, v110\n\t" \
  "v_max_u32 %[tv], %[tv], v111\n\t" \
  "v_cmp_eq_u32 vcc, -1, %[tv]\n\t" \
  "s_cbranch_vccnz .Lru11_%=\n\t" \
  ".Lbu11_%=:\n\t" \
  "s_waitcnt lgkmcnt(4)\n\t" \
  "v_mfma_f32_32x32x16_bf16 %[c0], v[88:91], v[108:111], %[c0]\n\t" \
  "v_mfma_f32_32x32x16_bf16 %[c1], v[92:95], v[108:111], %[c1]\n\t" \
  "global_load_dwordx4 v[88:91], %[lb], s[90:91] offset:-1024\n\t" \
  "global_load_dwordx4 v[92:95], %[lb], s[92:93] offset:-1024\n\t" \
  "s_waitcnt vmcnt(9)\n\t" \
  "v_max3_u32 %[tv], v112, v113, v114\n\t" \
  "v_max_u32 %[tv], %[tv], v115\n\t" \
  "v_cmp_eq_u32 vcc, -1, %[tv]\n\t" \
  "s_cbranch_vccnz .Lru12_%=\n\t" \
  ".Lbu12_%=:\n\t" \
  "s_waitcnt lgkmcnt(2)\n\t" \
  "v_mfma_f32_32x32x16_bf16 %[c0], v[48:51], v[112:115], %[c0]\n\t" \
  "v_mfma_f32_32x32x16_bf16 %[c1], v[52:55], v[112:115], %[c1]\n\t" \
  "global_load_dwordx4 v[48:51], %[lb], s[90:91] offset:0\n\t" \
  "global_load_dwordx4 v[52:55], %[lb], s[92:93] offset:0\n\t" \
  "s_waitcnt vmcnt(10)\n\t" \
  "v_max3_u32 %[tv], v116, v117, v118\n\t" \
  "v_max_u32 %[tv], %[tv], v119\n\t" \
  "v_cmp_eq_u32 vcc, -1, %[tv]\n\t" \
  "s_cbranch_vccnz .Lru13_%=\n\t" \
  ".Lbu13_%=:\n\t" \
  "s_waitcnt lgkmcnt(0)\n\t" \
  "v_mfma_f32_32x32x16_bf16 %[c0], v[56:59], v[116:119], %[c0]\n\t" \
  "v_mfma_f32_32x32x16_bf16 %[c1], v[60:63], v[116:119], %[c1]\n\t" \
  "global_load_dwordx4 v[56:59], %[lb], s[90:91] offset:1024\n\t" \
  "global_load_dwordx4 v[60:63], %[lb], s[92:93] offset:1024\n\t" \
  "s_setprio 3\n\t" \
  "s_mov_b64 s[94:95], %[aq]\n\t" \
  "s_cmp_lg_u32 %[wid], 0\n\t" \
  "s_cbranch_scc1 .Lqo%=\n\t" \
  "v_lshrrev_b32 v98, 2, %[l16]\n\t" \
  "s_mov_b32 %[t0], 0x40000\n\t" \
  ".Lpo%=:\n\t" \
  "global_load_dword v96, v98, %[fq] sc1\n\t" \
  "s_waitcnt vmcnt(0)\n\t" \
  "v_cmp_eq_u32 vcc, 0, v96\n\t" \
  "s_cbranch_vccz .Lqo%=\n\t" \
  "s_sleep 3\n\t" \
  "s_sub_u32 %[t0], %[t0], 1\n\t" \
  "s_cbranch_scc0 .Lpo%=\n\t" \
  ".Lqo%=:\n\t" \
  "s_barrier\n\t" \
  "global_load_dwordx4 v[96:99], %[l16], s[94:95] offset:-4096 sc1\n\t" \
  "global_load_dwordx4 v[100:103], %[l16], s[94:95] offset:-3072 sc1\n\t" \
  "global_load_dwordx4 v[104:107], %[l16], s[94:95] offset:-2048 sc1\n\t" \
  "global_load_dwordx4 v[108:111], %[l16], s[94:95] offset:-1024 sc1\n\t" \
  "global_load_dwordx4 v[112:115], %[l16], s[94:95] offset:0 sc1\n\t" \
  "global_load_dwordx4 v[116:119], %[l16], s[94:95] offset:1024 sc1\n\t" \
  "global_load_dwordx4 v[120:123], %[l16], s[94:95] offset:2048 sc1\n\t" \
  "global_load_dwordx4 v[124:127], %[l16], s[94:95] offset:3072 sc1\n\t" \
  "s_waitcnt vmcnt(7)\n\t" \
  "v_max3_u32 %[tv], v96, v97, v98\n\t" \
  "v_max_u32 %[tv], %[tv], v99\n\t" \
  "v_cmp_eq_u32 vcc, -1, %[tv]\n\t" \
  "s_cbranch_vccnz .Lro0_%=\n\t" \
  ".Lbo0_%=:\n\t" \
  "v_mfma_f32_32x32x16_bf16 %[c0], v[64:67], v[96:99], %[c0]\n\t" \
  "v_mfma_f32_32x32x16_bf16 %[c1], v[68:71], v[96:99], %[c1]\n\t" \
  "ds_read_b128 v[64:67], %[b0] offset:2048\n\t" \
  "ds_read_b128 v[68:71], %[b1] offset:2048\n\t" \
  "s_waitcnt vmcnt(6)\n\t" \
  "v_max3_u32 %[tv], v100, v101, v102\n\t" \
  "v_max_u32 %[tv], %[tv], v103\n\t" \
  "v_cmp_eq_u32 vcc, -1, %[tv]\n\t" \
  "s_cbranch_vccnz .Lro1_%=\n\t" \
  ".Lbo1_%=:\n\t" \
  "v_mfma_f32_32x32x16_bf16 %[c0], v[72:75], v[100:103], %[c0]\n\t" \
  "v_mfma_f32_32x32x16_bf16 %[c1], v[76:79], v[100:103], %[c1]\n\t" \
  "ds_read_b128 v[72:75], %[b0] offset:3072\n\t" \
  "ds_read_b128 v[76:79], %[b1] offset:3072\n\t" \
  "s_waitcnt vmcnt(5)\n\t" \
  "v_max3_u32 %[tv], v104, v105, v106\n\t" \
  "v_max_u32 %[tv], %[tv], v107\n\t" \
  "v_cmp_eq_u32 vcc, -1, %[tv]\n\t" \
  "s_cbranch_vccnz .Lro2_%=\n\t" \
  ".Lbo2_%=:\n\t" \
  "v_mfma_f32_32x32x16_bf16 %[c0], v[80:83], v[104:107], %[c0]\n\t" \
  "v_mfma_f32_32x32x16_bf16 %[c1], v[84:87], v[104:107], %[c1]\n\t" \
  "s_waitcnt vmcnt(4)\n\t" \
  "v_max3_u32 %[tv], v108, v109, v110\n\t" \
  "v_max_u32 %[tv], %[tv], v111\n\t" \
  "v_cmp_eq_u32 vcc, -1, %[tv]\n\t" \
  "s_cbranch_vccnz .Lro3_%=\n\t" \
  ".Lbo3_%=:\n\t" \
  "v_mfma_f32_32x32x16_bf16 %[c0], v[88:91], v[108:111], %[c0]\n\t" \
  "v_mfma_f32_32x32x16_bf16 %[c1], v[92:95], v[108:111], %[c1]\n\t" \
  "s_waitcnt vmcnt(3)\n\t" \
  "v_max3_u32 %[tv], v112, v113, v114\n\t" \
  "v_max_u32 %[tv], %[tv], v115\n\t" \
  "v_cmp_eq_u32 vcc, -1, %[tv]\n\t" \
  "s_cbranch_vccnz .Lro4_%=\n\t" \
  ".Lbo4_%=:\n\t" \
  "v_mfma_f32_32x32x16_bf16 %[c0], v[48:51], v[112:115], %[c0]\n\t" \
  "v_mfma_f32_32x32x16_bf16 %[c1], v[52:55], v[112:115], %[c1]\n\t" \
  "s_waitcnt vmcnt(2)\n\t" \
  "v_max3_u32 %[tv], v116, v117, v118\n\t" \
  "v_max_u32 %[tv], %[tv], v119\n\t" \
  "v_cmp_eq_u32 vcc, -1, %[tv]\n\t" \
  "s_cbranch_vccnz .Lro5_%=\n\t" \
  ".Lbo5_%=:\n\t" \
  "v_mfma_f32_32x32x16_bf16 %[c0], v[56:59], v[116:119], %[c0]\n\t" \
  "v_mfma_f32_32x32x16_bf16 %[c1], v[60:63], v[116:119], %[c1]\n\t" \
  "s_waitcnt vmcnt(1)\n\t" \
  "v_max3_u32 %[tv], v120, v121, v122\n\t" \
  "v_max_u32 %[tv], %[tv], v123\n\t" \
  "v_cmp_eq_u32 vcc, -1, %[tv]\n\t" \
  "s_cbranch_vccnz .Lro6_%=\n\t" \
  ".Lbo6_%=:\n\t" \
  "s_waitcnt lgkmcnt(2)\n\t" \
  "v_mfma_f32_32x32x16_bf16 %[c0], v[64:67], v[120:123], %[c0]\n\t" \
  "v_mfma_f32_32x32x16_bf16 %[c1], v[68:71], v[120:123], %[c1]\n\t" \
  "s_waitcnt vmcnt(0)\n\t" \
  "v_max3_u32 %[tv], v124, v125, v126\n\t" \
  "v_max_u32 %[tv], %[tv], v127\n\t" \
  "v_cmp_eq_u32 vcc, -1, %[tv]\n\t" \
  "s_cbranch_vccnz .Lro7_%=\n\t" \
  ".Lbo7_%=:\n\t" \
  "s_waitcnt lgkmcnt(0)\n\t" \
  "v_mfma_f32_32x32x16_bf16 %[c0], v[72:75], v[124:127], %[c0]\n\t" \
  "v_mfma_f32_32x32x16_bf16 %[c1], v[76:79], v[124:127], %[c1]\n\t" \
  "s_branch .Lend%=\n\t" \
  ".Lru0_%=:\n\t" \
  "s_cmp_eq_u32 s98, 0\n\t" \
  "s_cbranch_scc1 .Lbu0_%=\n\t" \
  "s_sub_u32 s98, s98, 1\n\t" \
  "s_sleep 1\n\t" \
  "global_load_dwordx4 v[96:99], %[l16], %[ap] offset:-4096 sc1\n\t" \
  "global_load_dwordx4 v[100:103], %[l16], %[ap] offset:-3072 sc1\n\t" \
  "global_load_dwordx4 v[104:107], %[l16], %[ap] offset:-2048 sc1\n\t" \
  "global_load_dwordx4 v[108:111], %[l16], %[ap] offset:-1024 sc1\n\t" \
  "s_waitcnt vmcnt(0)\n\t" \
  "v_max3_u32 %[tv], v96, v97, v98\n\t" \
  "v_max_u32 %[tv], %[tv], v99\n\t" \
  "v_cmp_eq_u32 vcc, -1, %[tv]\n\t" \
  "s_cbranch_vccnz .Lru0_%=\n\t" \
  "s_branch .Lbu0_%=\n\t" \
  ".Lru1_%=:\n\t" \
  "s_cmp_eq_u32 s98, 0\n\t" \
  "s_cbranch_scc1 .Lbu1_%=\n\t" \
  "s_sub_u32 s98, s98, 1\n\t" \
  "s_sleep 1\n\t" \
  "global_load_dwordx4 v[100:103], %[l16], %[ap] offset:-3072 sc1\n\t" \
  "global_load_dwordx4 v[104:107], %[l16], %[ap] offset:-2048 sc1\n\t" \
  "global_load_dwordx4 v[108:111], %[l16], %[ap] offset:-1024 sc1\n\t" \
  "global_load_dwordx4 v[112:115], %[l16], %[ap] offset:0 sc1\n\t" \
  "s_waitcnt vmcnt(0)\n\t" \
  "v_max3_u32 %[tv], v100, v101, v102\n\t" \
  "v_max_u32 %[tv], %[tv], v103\n\t" \
  "v_cmp_eq_u32 vcc, -1, %[tv]\n\t" \
  "s_cbranch_vccnz .Lru1_%=\n\t" \
  "s_branch .Lbu1_%=\n\t" \
  ".Lru2_%=:\n\t" \
  "s_cmp_eq_u32 s98, 0\n\t" \
  "s_cbranch_scc1 .Lbu2_%=\n\t" \
  "s_sub_u32 s98, s98, 1\n\t" \
  "s_sleep 1\n\t" \
  "global_load_dwordx4 v[104:107], %[l16], %[ap] offset:-2048 sc1\n\t" \
  "global_load_dwordx4 v[108:111], %[l16], %[ap] offset:-1024 sc1\n\t" \
  "global_load_dwordx4 v[112:115], %[l16], %[ap] offset:0 sc1\n\t" \
  "global_load_dwordx4 v[116:119], %[l16], %[ap] offset:1024 sc1\n\t" \
  "s_waitcnt vmcnt(0)\n\t" \
  "v_max3_u32 %[tv], v104, v105, v106\n\t" \
  "v_max_u32 %[tv], %[tv], v107\n\t" \
  "v_cmp_eq_u32 vcc, -1, %[tv]\n\t" \
  "s_cbranch_vccnz .Lru2_%=\n\t" \
  "s_branch .Lbu2_%=\n\t" \
  ".Lru3_%=:\n\t" \
  "s_cmp_eq_u32 s98, 0\n\t" \
  "s_cbranch_scc1 .Lbu3_%=\n\t" \
  "s_sub_u32 s98, s98, 1\n\t" \
  "s_sleep 1\n\t" \
  "global_load_dwordx4 v[108:111], %[l16], %[ap] offset:-1024 sc1\n\t" \
  "global_load_dwordx4 v[112:115], %[l16], %[ap] offset:0 sc1\n\t" \
  "global_load_dwordx4 v[116:119], %[l16], %[ap] offset:1024 sc1\n\t" \
  "global_load_dwordx4 v[120:123], %[l16], %[ap] offset:2048 sc1\n\t" \
  "s_waitcnt vmcnt(0)\n\t" \
  "v_max3_u32 %[tv], v108, v109, v110\n\t" \
  "v_max_u32 %[tv], %[tv], v111\n\t" \
  "v_cmp_eq_u32 vcc, -1, %[tv]\n\t" \
  "s_cbranch_vccnz .Lru3_%=\n\t" \
  "s_branch .Lbu3_%=\n\t" \
  ".Lru4_%=:\n\t" \
  "s_cmp_eq_u32 s98, 0\n\t" \
  "s_cbranch_scc1 .Lbu4_%=\n\t" \
  "s_sub_u32 s98, s98, 1\n\t" \
  "s_sleep 1\n\t" \
  "global_load_dwordx4 v[112:115], %[l16], %[ap] offset:0 sc1\n\t" \
  "global_load_dwordx4 v[116:119], %[l16], %[ap] offset:1024 sc1\n\t" \
  "global_load_dwordx4 v[120:123], %[l16], %[ap] offset:2048 sc1\n\t" \
  "global_load_dwordx4 v[124:127], %[l16], %[ap] offset:3072 sc1\n\t" \
  "s_waitcnt vmcnt(0)\n\t" \
  "v_max3_u32 %[tv], v112, v113, v114\n\t" \
  "v_max_u32 %[tv], %[tv], v115\n\t" \
  "v_cmp_eq_u32 vcc, -1, %[tv]\n\t" \
  "s_cbranch_vccnz .Lru4_%=\n\t" \
  "s_branch .Lbu4_%=\n\t" \
  ".Lru5_%=:\n\t" \
  "s_cmp_eq_u32 s98, 0\n\t" \
  "s_cbranch_scc1 .Lbu5_%=\n\t" \
  "s_sub_u32 s98, s98, 1\n\t" \
  "s_sleep 1\n\t" \
  "global_load_dwordx4 v[116:119], %[l16], %[ap] offset:1024 sc1\n\t" \
  "global_load_dwordx4 v[120:123], %[l16], %[ap] offset:2048 sc1\n\t" \
  "global_load_dwordx4 v[124:127], %[l16], %[ap] offset:3072 sc1\n\t" \
  "global_load_dwordx4 v[96:99], %[l16], s[94:95] offset:-4096 sc1\n\t" \
  "s_waitcnt vmcnt(0)\n\t" \
  "v_max3_u32 %[tv], v116, v117, v118\n\t" \
  "v_max_u32 %[tv], %[tv], v119\n\t" \
  "v_cmp_eq_u32 vcc, -1, %[tv]\n\t" \
  "s_cbranch_vccnz .Lru5_%=\n\t" \
  "s_branch .Lbu5_%=\n\t" \
  ".Lru6_%=:\n\t" \
  "s_cmp_eq_u32 s98, 0\n\t" \
  "s_cbranch_scc1 .Lbu6_%=\n\t" \
  "s_sub_u32 s98, s98, 1\n\t" \
  "s_sleep 1\n\t" \
  "global_load_dwordx4 v[120:123], %[l16], %[ap] offset:2048 sc1\n\t" \
  "global_load_dwordx4 v[124:127], %[l16], %[ap] offset:3072 sc1\n\t" \
  "global_load_dwordx4 v[96:99], %[l16], s[94:95] offset:-4096 sc1\n\t" \
  "global_load_dwordx4 v[100:103], %[l16], s[94:95] offset:-3072 sc1\n\t" \
  "s_waitcnt vmcnt(0)\n\t" \
  "v_max3_u32 %[tv], v120, v121, v122\n\t" \
  "v_max_u32 %[tv], %[tv], v123\n\t" \
  "v_cmp_eq_u32 vcc, -1, %[tv]\n\t" \
  "s_cbranch_vccnz .Lru6_%=\n\t" \
  "s_branch .Lbu6_%=\n\t" \
  ".Lru7_%=:\n\t" \
  "s_cmp_eq_u32 s98, 0\n\t" \
  "s_cbranch_scc1 .Lbu7_%=\n\t" \
  "s_sub_u32 s98, s98, 1\n\t" \
  "s_sleep 1\n\t" \
  "global_load_dwordx4 v[124:127], %[l16], %[ap] offset:3072 sc1\n\t" \
  "global_load_dwordx4 v[96:99], %[l16], s[94:95] offset:-4096 sc1\n\t" \
  "global_load_dwordx4 v[100:103], %[l16], s[94:95] offset:-3072 sc1\n\t" \
  "global_load_dwordx4 v[104:107], %[l16], s[94:95] offset:-2048 sc1\n\t" \
  "s_waitcnt vmcnt(0)\n\t" \
  "v_max3_u32 %[tv], v124, v125, v126\n\t" \
  "v_max_u32 %[tv], %[tv], v127\n\t" \
  "v_cmp_eq_u32 vcc, -1, %[tv]\n\t" \
  "s_cbranch_vccnz .Lru7_%=\n\t" \
  "s_branch .Lbu7_%=\n\t" \
  ".Lru8_%=:\n\t" \
  "s_cmp_eq_u32 s98, 0\n\t" \
  "s_cbranch_scc1 .Lbu8_%=\n\t" \
  "s_sub_u32 s98, s98, 1\n\t" \
  "s_sleep 1\n\t" \
  "global_load_dwordx4 v[96:99], %[l16], s[94:95] offset:-4096 sc1\n\t" \
  "global_load_dwordx4 v[100:103], %[l16], s[94:95] offset:-3072 sc1\n\t" \
  "global_load_dwordx4 v[104:107], %[l16], s[94:95] offset:-2048 sc1\n\t" \
  "global_load_dwordx4 v[108:111], %[l16], s[94:95] offset:-1024 sc1\n\t" \
  "s_waitcnt vmcnt(0)\n\t" \
  "v_max3_u32 %[tv], v96, v97, v98\n\t" \
  "v_max_u32 %[tv], %[tv], v99\n\t" \
  "v_cmp_eq_u32 vcc, -1, %[tv]\n\t" \
  "s_cbranch_vccnz .Lru8_%=\n\t" \
  "s_branch .Lbu8_%=\n\t" \
  ".Lru9_%=:\n\t" \
  "s_cmp_eq_u32 s98, 0\n\t" \
  "s_cbranch_scc1 .Lbu9_%=\n\t" \
  "s_sub_u32 s98, s98, 1\n\t" \
  "s_sleep 1\n\t" \
  "global_load_dwordx4 v[100:103], %[l16], s[94:95] offset:-3072 sc1\n\t" \
  "global_load_dwordx4 v[104:107], %[l16], s[94:95] offset:-2048 sc1\n\t" \
  "global_load_dwordx4 v[108:111], %[l16], s[94:95] offset:-1024 sc1\n\t" \
  "global_load_dwordx4 v[112:115], %[l16], s[94:95] offset:0 sc1\n\t" \
  "s_waitcnt vmcnt(0)\n\t" \
  "v_max3_u32 %[tv], v100, v101, v102\n\t" \
  "v_max_u32 %[tv], %[tv], v103\n\t" \
  "v_cmp_eq_u32 vcc, -1, %[tv]\n\t" \
  "s_cbranch_vccnz .Lru9_%=\n\t" \
  "s_branch .Lbu9_%=\n\t" \
  ".Lru10_%=:\n\t" \
  "s_cmp_eq_u32 s98, 0\n\t" \
  "s_cbranch_scc1 .Lbu10_%=\n\t" \
  "s_sub_u32 s98, s98, 1\n\t" \
  "s_sleep 1\n\t" \
  "global_load_dwordx4 v[104:107], %[l16], s[94:95] offset:-2048 sc1\n\t" \
  "global_load_dwordx4 v[108:111], %[l16], s[94:95] offset:-1024 sc1\n\t" \
  "global_load_dwordx4 v[112:115], %[l16], s[94:95] offset:0 sc1\n\t" \
  "global_load_dwordx4 v[116:119], %[l16], s[94:95] offset:1024 sc1\n\t" \
  "s_waitcnt vmcnt(0)\n\t" \
  "v_max3_u32 %[tv], v104, v105, v106\n\t" \
  "v_max_u32 %[tv], %[tv], v107\n\t" \
  "v_cmp_eq_u32 vcc, -1, %[tv]\n\t" \
  "s_cbranch_vccnz .Lru10_%=\n\t" \
  "s_branch .Lbu10_%=\n\t" \
  ".Lru11_%=:\n\t" \
  "s_cmp_eq_u32 s98, 0\n\t" \
  "s_cbranch_scc1 .Lbu11_%=\n\t" \
  "s_sub_u32 s98, s98, 1\n\t" \
  "s_sleep 1\n\t" \
  "global_load_dwordx4 v[108:111], %[l16], s[94:95] offset:-1024 sc1\n\t" \
  "global_load_dwordx4 v[112:115], %[l16], s[94:95] offset:0 sc1\n\t" \
  "global_load_dwordx4 v[116:119], %[l16], s[94:95] offset:1024 sc1\n\t" \
  "s_waitcnt vmcnt(0)\n\t" \
  "v_max3_u32 %[tv], v108, v109, v110\n\t" \
  "v_max_u32 %[tv], %[tv], v111\n\t" \
  "v_cmp_eq_u32 vcc, -1, %[tv]\n\t" \
  "s_cbranch_vccnz .Lru11_%=\n\t" \
  "s_branch .Lbu11_%=\n\t" \
  ".Lru12_%=:\n\t" \
  "s_cmp_eq_u32 s98, 0\n\t" \
  "s_cbranch_scc1 .Lbu12_%=\n\t" \
  "s_sub_u32 s98, s98, 1\n\t" \
  "s_sleep 1\n\t" \
  "global_load_dwordx4 v[112:115], %[l16], s[94:95] offset:0 sc1\n\t" \
  "global_load_dwordx4 v[116:119], %[l16], s[94:95] offset:1024 sc1\n\t" \
  "s_waitcnt vmcnt(0)\n\t" \
  "v_max3_u32 %[tv], v112, v113, v114\n\t" \
  "v_max_u32 %[tv], %[tv], v115\n\t" \
  "v_cmp_eq_u32 vcc, -1, %[tv]\n\t" \
  "s_cbranch_vccnz .Lru12_%=\n\t" \
  "s_branch .Lbu12_%=\n\t" \
  ".Lru13_%=:\n\t" \
  "s_cmp_eq_u32 s98, 0\n\t" \
  "s_cbranch_scc1 .Lbu13_%=\n\t" \
  "s_sub_u32 s98, s98, 1\n\t" \
  "s_sleep 1\n\t" \
  "global_load_dwordx4 v[116:119], %[l16], s[94:95] offset:1024 sc1\n\t" \
  "s_waitcnt vmcnt(0)\n\t" \
  "v_max3_u32 %[tv], v116, v117, v118\n\t" \
  "v_max_u32 %[tv], %[tv], v119\n\t" \
  "v_cmp_eq_u32 vcc, -1, %[tv]\n\t" \
  "s_cbranch_vccnz .Lru13_%=\n\t" \
  "s_branch .Lbu13_%=\n\t" \
  ".Lro0_%=:\n\t" \
  "s_cmp_eq_u32 s98, 0\n\t" \
  "s_cbranch_scc1 .Lbo0_%=\n\t" \
  "s_sub_u32 s98, s98, 1\n\t" \
  "s_sleep 1\n\t" \
  "global_load_dwordx4 v[96:99], %[l16], %[aq] offset:-4096 sc1\n\t" \
  "global_load_dwordx4 v[100:103], %[l16], %[aq] offset:-3072 sc1\n\t" \
  "global_load_dwordx4 v[104:107], %[l16], %[aq] offset:-2048 sc1\n\t" \
  "global_load_dwordx4 v[108:111], %[l16], %[aq] offset:-1024 sc1\n\t" \
  "s_waitcnt vmcnt(0)\n\t" \
  "v_max3_u32 %[tv], v96, v97, v98\n\t" \
  "v_max_u32 %[tv], %[tv], v99\n\t" \
  "v_cmp_eq_u32 vcc, -1, %[tv]\n\t" \
  "s_cbranch_vccnz .Lro0_%=\n\t" \
  "s_branch .Lbo0_%=\n\t" \
  ".Lro1_%=:\n\t" \
  "s_cmp_eq_u32 s98, 0\n\t" \
  "s_cbranch_scc1 .Lbo1_%=\n\t" \
  "s_sub_u32 s98, s98, 1\n\t" \
  "s_sleep 1\n\t" \
  "global_load_dwordx4 v[100:103], %[l16], %[aq] offset:-3072 sc1\n\t" \
  "global_load_dwordx4 v[104:107], %[l16], %[aq] offset:-2048 sc1\n\t" \
  "global_load_dwordx4 v[108:111], %[l16], %[aq] offset:-1024 sc1\n\t" \
  "global_load_dwordx4 v[112:115], %[l16], %[aq] offset:0 sc1\n\t" \
  "s_waitcnt vmcnt(0)\n\t" \
  "v_max3_u32 %[tv], v100, v101, v102\n\t" \
  "v_max_u32 %[tv], %[tv], v103\n\t" \
  "v_cmp_eq_u32 vcc, -1, %[tv]\n\t" \
  "s_cbranch_vccnz .Lro1_%=\n\t" \
  "s_branch .Lbo1_%=\n\t" \
  ".Lro2_%=:\n\t" \
  "s_cmp_eq_u32 s98, 0\n\t" \
  "s_cbranch_scc1 .Lbo2_%=\n\t" \
  "s_sub_u32 s98, s98, 1\n\t" \
  "s_sleep 1\n\t" \
  "global_load_dwordx4 v[104:107], %[l16], %[aq] offset:-2048 sc1\n\t" \
  "global_load_dwordx4 v[108:111], %[l16], %[aq] offset:-1024 sc1\n\t" \
  "global_load_dwordx4 v[112:115], %[l16], %[aq] offset:0 sc1\n\t" \
  "global_load_dwordx4 v[116:119], %[l16], %[aq] offset:1024 sc1\n\t" \
  "s_waitcnt vmcnt(0)\n\t" \
  "v_max3_u32 %[tv], v104, v105, v106\n\t" \
  "v_max_u32 %[tv], %[tv], v107\n\t" \
  "v_cmp_eq_u32 vcc, -1, %[tv]\n\t" \
  "s_cbranch_vccnz .Lro2_%=\n\t" \
  "s_branch .Lbo2_%=\n\t" \
  ".Lro3_%=:\n\t" \
  "s_cmp_eq_u32 s98, 0\n\t" \
  "s_cbranch_scc1 .Lbo3_%=\n\t" \
  "s_sub_u32 s98, s98, 1\n\t" \
  "s_sleep 1\n\t" \
  "global_load_dwordx4 v[108:111], %[l16], %[aq] offset:-1024 sc1\n\t" \
  "global_load_dwordx4 v[112:115], %[l16], %[aq] offset:0 sc1\n\t" \
  "global_load_dwordx4 v[116:119], %[l16], %[aq] offset:1024 sc1\n\t" \
  "global_load_dwordx4 v[120:123], %[l16], %[aq] offset:2048 sc1\n\t" \
  "s_waitcnt vmcnt(0)\n\t" \
  "v_max3_u32 %[tv], v108, v109, v110\n\t" \
  "v_max_u32 %[tv], %[tv], v111\n\t" \
  "v_cmp_eq_u32 vcc, -1, %[tv]\n\t" \
  "s_cbranch_vccnz .Lro3_%=\n\t" \
  "s_branch .Lbo3_%=\n\t" \
  ".Lro4_%=:\n\t" \
  "s_cmp_eq_u32 s98, 0\n\t" \
  "s_cbranch_scc1 .Lbo4_%=\n\t" \
  "s_sub_u32 s98, s98, 1\n\t" \
  "s_sleep 1\n\t" \
  "global_load_dwordx4 v[112:115], %[l16], %[aq] offset:0 sc1\n\t" \
  "global_load_dwordx4 v[116:119], %[l16], %[aq] offset:1024 sc1\n\t" \
  "global_load_dwordx4 v[120:123], %[l16], %[aq] offset:2048 sc1\n\t" \
  "global_load_dwordx4 v[124:127], %[l16], %[aq] offset:3072 sc1\n\t" \
  "s_waitcnt vmcnt(0)\n\t" \
  "v_max3_u32 %[tv], v112, v113, v114\n\t" \
  "v_max_u32 %[tv], %[tv], v115\n\t" \
  "v_cmp_eq_u32 vcc, -1, %[tv]\n\t" \
  "s_cbranch_vccnz .Lro4_%=\n\t" \
  "s_branch .Lbo4_%=\n\t" \
  ".Lro5_%=:\n\t" \
  "s_cmp_eq_u32 s98, 0\n\t" \
  "s_cbranch_scc1 .Lbo5_%=\n\t" \
  "s_sub_u32 s98, s98, 1\n\t" \
  "s_sleep 1\n\t" \
  "global_load_dwordx4 v[116:119], %[l16], %[aq] offset:1024 sc1\n\t" \
  "global_load_dwordx4 v[120:123], %[l16], %[aq] offset:2048 sc1\n\t" \
  "global_load_dwordx4 v[124:127], %[l16], %[aq] offset:3072 sc1\n\t" \
  "s_waitcnt vmcnt(0)\n\t" \
  "v_max3_u32 %[tv], v116, v117, v118\n\t" \
  "v_max_u32 %[tv], %[tv], v119\n\t" \
  "v_cmp_eq_u32 vcc, -1, %[tv]\n\t" \
  "s_cbranch_vccnz .Lro5_%=\n\t" \
  "s_branch .Lbo5_%=\n\t" \
  ".Lro6_%=:\n\t" \
  "s_cmp_eq_u32 s98, 0\n\t" \
  "s_cbranch_scc1 .Lbo6_%=\n\t" \
  "s_sub_u32 s98, s98, 1\n\t" \
  "s_sleep 1\n\t" \
  "global_load_dwordx4 v[120:123], %[l16], %[aq] offset:2048 sc1\n\t" \
  "global_load_dwordx4 v[124:127], %[l16], %[aq] offset:3072 sc1\n\t" \
  "s_waitcnt vmcnt(0)\n\t" \
  "v_max3_u32 %[tv], v120, v121, v122\n\t" \
  "v_max_u32 %[tv], %[tv], v123\n\t" \
  "v_cmp_eq_u32 vcc, -1, %[tv]\n\t" \
  "s_cbranch_vccnz .Lro6_%=\n\t" \
  "s_branch .Lbo6_%=\n\t" \
  ".Lro7_%=:\n\t" \
  "s_cmp_eq_u32 s98, 0\n\t" \
  "s_cbranch_scc1 .Lbo7_%=\n\t" \
  "s_sub_u32 s98, s98, 1\n\t" \
  "s_sleep 1\n\t" \
  "global_load_dwordx4 v[124:127], %[l16], %[aq] offset:3072 sc1\n\t" \
  "s_waitcnt vmcnt(0)\n\t" \
  "v_max3_u32 %[tv], v124, v125, v126\n\t" \
  "v_max_u32 %[tv], %[tv], v127\n\t" \
  "v_cmp_eq_u32 vcc, -1, %[tv]\n\t" \
  "s_cbranch_vccnz .Lro7_%=\n\t" \
  "s_branch .Lbo7_%=\n\t" \
  ".Lend%=:\n\t" \
  "s_nop 15\n\t" \
  ""
#define G_CLOB48 "v48","v49","v50","v51","v52","v53","v54","v55","v56","v57","v58","v59","v60","v61","v62","v63"
#define G_SCLOB "s90","s91","s92","s93","s94","s95","s96","s97","s98","s99"
#define K1_LOOP \
  "s_cmp_lg_u32 %[z], 0\n\t" \
  "s_cbranch_scc1 .Lz%=\n\t" \
  "ds_read_b128 v[64:67], %[a0]\n\t" \
  "ds_read_b64_tr_b16 v[68:69], %[b] offset:0\n\t" \
  "ds_read_b64_tr_b16 v[70:71], %[b] offset:512\n\t" \
  "ds_read_b64_tr_b16 v[72:73], %[b] offset:2048\n\t" \
  "ds_read_b64_tr_b16 v[74:75], %[b] offset:2560\n\t" \
  "ds_read_b64_tr_b16 v[76:77], %[b] offset:4096\n\t" \
  "ds_read_b64_tr_b16 v[78:79], %[b] offset:4608\n\t" \
  "ds_read_b128 v[80:83], %[a1]\n\t" \
  "ds_read_b64_tr_b16 v[84:85], %[b] offset:6144\n\t" \
  "ds_read_b64_tr_b16 v[86:87], %[b] offset:6656\n\t" \
  "ds_read_b64_tr_b16 v[88:89], %[b] offset:8192\n\t" \
  "ds_read_b64_tr_b16 v[90:91], %[b] offset:8704\n\t" \
  "ds_read_b64_tr_b16 v[92:93], %[b] offset:10240\n\t" \
  "ds_read_b64_tr_b16 v[94:95], %[b] offset:10752\n\t" \
  "s_mov_b32 %[i], 0\n\t" \
  "s_mov_b32 %[sl], 0\n\t" \
  ".Lk3%=:\n\t" \
  "s_mov_b32 %[d], 40960\n\t" \
  "s_add_u32 %[sl], %[sl], 1\n\t" \
  "s_cmp_eq_u32 %[sl], 3\n\t" \
  "s_cselect_b32 %[d], 0xfffec000, %[d]\n\t" \
  "s_cselect_b32 %[sl], 0, %[sl]\n\t" \
  "s_waitcnt lgkmcnt(11)\n\t" \
  "v_mfma_f32_32x32x16_bf16 %[c0], v[68:71], v[64:67], %[c0]\n\t" \
  "ds_read_b128 v[96:99], %[a2]\n\t" \
  "ds_read_b64_tr_b16 v[100:101], %[b] offset:12288\n\t" \
  "ds_read_b64_tr_b16 v[102:103], %[b] offset:12800\n\t" \
  "v_add_u32 %[a0], %[d], %[a0]\n\t" \
  "s_waitcnt lgkmcnt(12)\n\t" \
  "v_mfma_f32_32x32x16_bf16 %[c1], v[72:75], v[64:67], %[c1]\n\t" \
  "ds_read_b64_tr_b16 v[104:105], %[b] offset:14336\n\t" \
  "ds_read_b64_tr_b16 v[106:107], %[b] offset:14848\n\t" \
  "ds_read_b64_tr_b16 v[108:109], %[b] offset:16384\n\t" \
  "v_add_u32 %[a1], %[d], %[a1]\n\t" \
  "s_waitcnt lgkmcnt(13)\n\t" \
  "v_mfma_f32_32x32x16_bf16 %[c2], v[64:67], v[76:79], %[c2]\n\t" \
  "ds_read_b64_tr_b16 v[110:111], %[b] offset:16896\n\t" \
  "s_waitcnt lgkmcnt(11)\n\t" \
  "v_mfma_f32_32x32x16_bf16 %[c0], v[84:87], v[80:83], %[c0]\n\t" \
  "ds_read_b128 v[112:115], %[a3]\n\t" \
  "ds_read_b64_tr_b16 v[116:117], %[b] offset:18432\n\t" \
  "ds_read_b64_tr_b16 v[118:119], %[b] offset:18944\n\t" \
  "s_waitcnt lgkmcnt(12)\n\t" \
  "v_mfma_f32_32x32x16_bf16 %[c1], v[88:91], v[80:83], %[c1]\n\t" \
  "ds_read_b64_tr_b16 v[120:121], %[b] offset:20480\n\t" \
  "ds_read_b64_tr_b16 v[122:123], %[b] offset:20992\n\t" \
  "ds_read_b64_tr_b16 v[124:125], %[b] offset:22528\n\t" \
  "s_waitcnt lgkmcnt(13)\n\t" \
  "v_mfma_f32_32x32x16_bf16 %[c2], v[80:83], v[92:95], %[c2]\n\t" \
  "ds_read_b64_tr_b16 v[126:127], %[b] offset:23040\n\t" \
  "s_waitcnt lgkmcnt(11)\n\t" \
  "v_mfma_f32_32x32x16_bf16 %[c0], v[100:103], v[96:99], %[c0]\n\t" \
  "v_add_u32 %[a2], %[d], %[a2]\n\t" \
  "s_waitcnt lgkmcnt(0)\n\t" \
  "v_add_u32 %[b], %[d], %[b]\n\t" \
  "s_barrier\n\t" \
  "v_mfma_f32_32x32x16_bf16 %[c1], v[104:107], v[96:99], %[c1]\n\t" \
  "ds_read_b128 v[64:67], %[a0]\n\t" \
  "ds_read_b64_tr_b16 v[68:69], %[b] offset:0\n\t" \
  "ds_read_b64_tr_b16 v[70:71], %[b] offset:512\n\t" \
  "ds_read_b64_tr_b16 v[72:73], %[b] offset:2048\n\t" \
  "v_add_u32 %[a3], %[d], %[a3]\n\t" \
  "v_mfma_f32_32x32x16_bf16 %[c2], v[96:99], v[108:111], %[c2]\n\t" \
  "ds_read_b64_tr_b16 v[74:75], %[b] offset:2560\n\t" \
  "ds_read_b64_tr_b16 v[76:77], %[b] offset:4096\n\t" \
  "ds_read_b64_tr_b16 v[78:79], %[b] offset:4608\n\t" \
  "v_mfma_f32_32x32x16_bf16 %[c0], v[116:119], v[112:115], %[c0]\n\t" \
  "ds_read_b128 v[80:83], %[a1]\n\t" \
  "ds_read_b64_tr_b16 v[84:85], %[b] offset:6144\n\t" \
  "ds_read_b64_tr_b16 v[86:87], %[b] offset:6656\n\t" \
  "v_mfma_f32_32x32x16_bf16 %[c1], v[120:123], v[112:115], %[c1]\n\t" \
  "ds_read_b64_tr_b16 v[88:89], %[b] offset:8192\n\t" \
  "ds_read_b64_tr_b16 v[90:91], %[b] offset:8704\n\t" \
  "ds_read_b64_tr_b16 v[92:93], %[b] offset:10240\n\t" \
  "v_mfma_f32_32x32x16_bf16 %[c2], v[112:115], v[124:127], %[c2]\n\t" \
  "ds_read_b64_tr_b16 v[94:95], %[b] offset:10752\n\t" \
  "s_add_u32 %[i], %[i], 1\n\t" \
  "s_cmp_lt_u32 %[i], 8\n\t" \
  "s_cbranch_scc1 .Lk3%=\n\t" \
  "s_branch .Le%=\n\t" \
  ".Lz%=:\n\t" \
  "ds_read_b128 v[64:67], %[a0]\n\t" \
  "ds_read_b64_tr_b16 v[68:69], %[b] offset:0\n\t" \
  "ds_read_b64_tr_b16 v[70:71], %[b] offset:512\n\t" \
  "ds_read_b64_tr_b16 v[72:73], %[b] offset:2048\n\t" \
  "ds_read_b64_tr_b16 v[74:75], %[b] offset:2560\n\t" \
  "ds_read_b128 v[80:83], %[a1]\n\t" \
  "ds_read_b64_tr_b16 v[84:85], %[b] offset:6144\n\t" \
  "ds_read_b64_tr_b16 v[86:87], %[b] offset:6656\n\t" \
  "ds_read_b64_tr_b16 v[88:89], %[b] offset:8192\n\t" \
  "ds_read_b64_tr_b16 v[90:91], %[b] offset:8704\n\t" \
  "s_mov_b32 %[i], 0\n\t" \
  "s_mov_b32 %[sl], 0\n\t" \
  ".Lk2%=:\n\t" \
  "s_mov_b32 %[d], 40960\n\t" \
  "s_add_u32 %[sl], %[sl], 1\n\t" \
  "s_cmp_eq_u32 %[sl], 3\n\t" \
  "s_cselect_b32 %[d], 0xfffec000, %[d]\n\t" \
  "s_cselect_b32 %[sl], 0, %[sl]\n\t" \
  "s_waitcnt lgkmcnt(7)\n\t" \
  "v_mfma_f32_32x32x16_bf16 %[c0], v[68:71], v[64:67], %[c0]\n\t" \
  "ds_read_b128 v[96:99], %[a2]\n\t" \
  "ds_read_b64_tr_b16 v[100:101], %[b] offset:12288\n\t" \
  "ds_read_b64_tr_b16 v[102:103], %[b] offset:12800\n\t" \
  "v_add_u32 %[a0], %[d], %[a0]\n\t" \
  "s_waitcnt lgkmcnt(8)\n\t" \
  "v_mfma_f32_32x32x16_bf16 %[c1], v[72:75], v[64:67], %[c1]\n\t" \
  "ds_read_b64_tr_b16 v[104:105], %[b] offset:14336\n\t" \
  "ds_read_b64_tr_b16 v[106:107], %[b] offset:14848\n\t" \
  "v_add_u32 %[a1], %[d], %[a1]\n\t" \
  "s_waitcnt lgkmcnt(7)\n\t" \
  "v_mfma_f32_32x32x16_bf16 %[c0], v[84:87], v[80:83], %[c0]\n\t" \
  "ds_read_b128 v[112:115], %[a3]\n\t" \
  "ds_read_b64_tr_b16 v[116:117], %[b] offset:18432\n\t" \
  "ds_read_b64_tr_b16 v[118:119], %[b] offset:18944\n\t" \
  "s_waitcnt lgkmcnt(8)\n\t" \
  "v_mfma_f32_32x32x16_bf16 %[c1], v[88:91], v[80:83], %[c1]\n\t" \
  "ds_read_b64_tr_b16 v[120:121], %[b] offset:20480\n\t" \
  "ds_read_b64_tr_b16 v[122:123], %[b] offset:20992\n\t" \
  "s_waitcnt lgkmcnt(7)\n\t" \
  "v_mfma_f32_32x32x16_bf16 %[c0], v[100:103], v[96:99], %[c0]\n\t" \
  "v_add_u32 %[a2], %[d], %[a2]\n\t" \
  "s_waitcnt lgkmcnt(0)\n\t" \
  "v_add_u32 %[b], %[d], %[b]\n\t" \
  "s_barrier\n\t" \
  "v_mfma_f32_32x32x16_bf16 %[c1], v[104:107], v[96:99], %[c1]\n\t" \
  "ds_read_b128 v[64:67], %[a0]\n\t" \
  "ds_read_b64_tr_b16 v[68:69], %[b] offset:0\n\t" \
  "ds_read_b64_tr_b16 v[70:71], %[b] offset:512\n\t" \
  "ds_read_b64_tr_b16 v[72:73], %[b] offset:2048\n\t" \
  "ds_read_b64_tr_b16 v[74:75], %[b] offset:2560\n\t" \
  "v_add_u32 %[a3], %[d], %[a3]\n\t" \
  "v_mfma_f32_32x32x16_bf16 %[c0], v[116:119], v[112:115], %[c0]\n\t" \
  "ds_read_b128 v[80:83], %[a1]\n\t" \
  "ds_read_b64_tr_b16 v[84:85], %[b] offset:6144\n\t" \
  "ds_read_b64_tr_b16 v[86:87], %[b] offset:6656\n\t" \
  "v_mfma_f32_32x32x16_bf16 %[c1], v[120:123], v[112:115], %[c1]\n\t" \
  "ds_read_b64_tr_b16 v[88:89], %[b] offset:8192\n\t" \
  "ds_read_b64_tr_b16 v[90:91], %[b] offset:8704\n\t" \
  "s_add_u32 %[i], %[i], 1\n\t" \
  "s_cmp_lt_u32 %[i], 8\n\t" \
  "s_cbranch_scc1 .Lk2%=\n\t" \
  ".Le%=:\n\t" \
  "s_waitcnt lgkmcnt(0)\n\t" \
  "s_nop 10\n\t" \
  ""
#define K1_HLOOP \
  "s_mov_b32 %[i], 0\n\t" \
  "s_mov_b32 %[sl], 0\n\t" \
  ".Lh%=:\n\t" \
  "s_barrier\n\t" \
  "s_cmp_lg_u32 %[i], %[keep]\n\t" \
  "s_cbranch_scc1 .Lhk%=\n\t" \
  "ds_read_b128 %[x0], %[a0]\n\t" \
  "ds_read_b128 %[x1], %[a1]\n\t" \
  "ds_read_b128 %[x2], %[a2]\n\t" \
  "ds_read_b128 %[x3], %[a3]\n\t" \
  "s_waitcnt lgkmcnt(0)\n\t" \
  ".Lhk%=:\n\t" \
  "s_cmp_gt_u32 %[cs], 2\n\t" \
  "s_cbranch_scc1 .Lhn%=\n\t" \
  "ds_read_b128 v[64:67], %[a0]\n\t" \
  "ds_read_b128 v[68:71], %[a1]\n\t" \
  "ds_read_b128 v[72:75], %[a2]\n\t" \
  "ds_read_b128 v[76:79], %[a3]\n\t" \
  "ds_read_b64_tr_b16 v[80:81], %[tb] offset:0\n\t" \
  "ds_read_b64_tr_b16 v[82:83], %[tb] offset:512\n\t" \
  "ds_read_b64_tr_b16 v[84:85], %[tc] offset:0\n\t" \
  "ds_read_b64_tr_b16 v[86:87], %[tc] offset:512\n\t" \
  "ds_read_b64_tr_b16 v[88:89], %[tb] offset:6144\n\t" \
  "ds_read_b64_tr_b16 v[90:91], %[tb] offset:6656\n\t" \
  "ds_read_b64_tr_b16 v[92:93], %[tc] offset:6144\n\t" \
  "ds_read_b64_tr_b16 v[94:95], %[tc] offset:6656\n\t" \
  "s_waitcnt lgkmcnt(7)\n\t" \
  "ds_read_b64_tr_b16 v[96:97], %[tb] offset:12288\n\t" \
  "ds_read_b64_tr_b16 v[98:99], %[tb] offset:12800\n\t" \
  "ds_read_b64_tr_b16 v[100:101], %[tc] offset:12288\n\t" \
  "ds_read_b64_tr_b16 v[102:103], %[tc] offset:12800\n\t" \
  "ds_read_b64_tr_b16 v[104:105], %[tb] offset:18432\n\t" \
  "ds_read_b64_tr_b16 v[106:107], %[tb] offset:18944\n\t" \
  "ds_read_b64_tr_b16 v[108:109], %[tc] offset:18432\n\t" \
  "ds_read_b64_tr_b16 v[110:111], %[tc] offset:18944\n\t" \
  "v_lshlrev_b32 v112, 16, v64\n\t" \
  "v_and_b32 v113, %[msk], v64\n\t" \
  "v_lshlrev_b32 v114, 16, v65\n\t" \
  "v_and_b32 v115, %[msk], v65\n\t" \
  "v_fmac_f32 %[qx], v112, v112\n\t" \
  "v_fmac_f32 %[qy], v113, v113\n\t" \
  "v_fmac_f32 %[qz], v114, v114\n\t" \
  "v_fmac_f32 %[qw], v115, v115\n\t" \
  "v_lshlrev_b32 v112, 16, v66\n\t" \
  "v_and_b32 v113, %[msk], v66\n\t" \
  "v_lshlrev_b32 v114, 16, v67\n\t" \
  "v_and_b32 v115, %[msk], v67\n\t" \
  "v_fmac_f32 %[qx], v112, v112\n\t" \
  "v_fmac_f32 %[qy], v113, v113\n\t" \
  "v_fmac_f32 %[qz], v114, v114\n\t" \
  "v_fmac_f32 %[qw], v115, v115\n\t" \
  "v_lshlrev_b32 v112, 16, v68\n\t" \
  "v_and_b32 v113, %[msk], v68\n\t" \
  "v_lshlrev_b32 v114, 16, v69\n\t" \
  "v_and_b32 v115, %[msk], v69\n\t" \
  "v_fmac_f32 %[qx], v112, v112\n\t" \
  "v_fmac_f32 %[qy], v113, v113\n\t" \
  "v_fmac_f32 %[qz], v114, v114\n\t" \
  "v_fmac_f32 %[qw], v115, v115\n\t" \
  "v_lshlrev_b32 v112, 16, v70\n\t" \
  "v_and_b32 v113, %[msk], v70\n\t" \
  "v_lshlrev_b32 v114, 16, v71\n\t" \
  "v_and_b32 v115, %[msk], v71\n\t" \
  "v_fmac_f32 %[qx], v112, v112\n\t" \
  "v_fmac_f32 %[qy], v113, v113\n\t" \
  "v_fmac_f32 %[qz], v114, v114\n\t" \
  "v_fmac_f32 %[qw], v115, v115\n\t" \
  "s_waitcnt lgkmcnt(0)\n\t" \
  "s_setprio 3\n\t" \
  "v_mfma_f32_16x16x32_bf16 %[sr], %[sel], v[64:67], %[sr]\n\t" \
  "v_mfma_f32_16x16x32_bf16 %[s0], %[sel], v[80:83], %[s0]\n\t" \
  "v_mfma_f32_16x16x32_bf16 %[s1], %[sel], v[84:87], %[s1]\n\t" \
  "v_mfma_f32_16x16x32_bf16 %[sr], %[sel], v[68:71], %[sr]\n\t" \
  "v_mfma_f32_16x16x32_bf16 %[s0], %[sel], v[88:91], %[s0]\n\t" \
  "v_mfma_f32_16x16x32_bf16 %[s1], %[sel], v[92:95], %[s1]\n\t" \
  "v_mfma_f32_16x16x32_bf16 %[sr], %[sel], v[72:75], %[sr]\n\t" \
  "v_mfma_f32_16x16x32_bf16 %[s0], %[sel], v[96:99], %[s0]\n\t" \
  "v_mfma_f32_16x16x32_bf16 %[s1], %[sel], v[100:103], %[s1]\n\t" \
  "v_mfma_f32_16x16x32_bf16 %[sr], %[sel], v[76:79], %[sr]\n\t" \
  "v_mfma_f32_16x16x32_bf16 %[s0], %[sel], v[104:107], %[s0]\n\t" \
  "v_mfma_f32_16x16x32_bf16 %[s1], %[sel], v[108:111], %[s1]\n\t" \
  "s_setprio 2\n\t" \
  "v_lshlrev_b32 v112, 16, v72\n\t" \
  "v_and_b32 v113, %[msk], v72\n\t" \
  "v_lshlrev_b32 v114, 16, v73\n\t" \
  "v_and_b32 v115, %[msk], v73\n\t" \
  "v_fmac_f32 %[qx], v112, v112\n\t" \
  "v_fmac_f32 %[qy], v113, v113\n\t" \
  "v_fmac_f32 %[qz], v114, v114\n\t" \
  "v_fmac_f32 %[qw], v115, v115\n\t" \
  "v_lshlrev_b32 v112, 16, v74\n\t" \
  "v_and_b32 v113, %[msk], v74\n\t" \
  "v_lshlrev_b32 v114, 16, v75\n\t" \
  "v_and_b32 v115, %[msk], v75\n\t" \
  "v_fmac_f32 %[qx], v112, v112\n\t" \
  "v_fmac_f32 %[qy], v113, v113\n\t" \
  "v_fmac_f32 %[qz], v114, v114\n\t" \
  "v_fmac_f32 %[qw], v115, v115\n\t" \
  "v_lshlrev_b32 v112, 16, v76\n\t" \
  "v_and_b32 v113, %[msk], v76\n\t" \
  "v_lshlrev_b32 v114, 16, v77\n\t" \
  "v_and_b32 v115, %[msk], v77\n\t" \
  "v_fmac_f32 %[qx], v112, v112\n\t" \
  "v_fmac_f32 %[qy], v113, v113\n\t" \
  "v_fmac_f32 %[qz], v114, v114\n\t" \
  "v_fmac_f32 %[qw], v115, v115\n\t" \
  "v_lshlrev_b32 v112, 16, v78\n\t" \
  "v_and_b32 v113, %[msk], v78\n\t" \
  "v_lshlrev_b32 v114, 16, v79\n\t" \
  "v_and_b32 v115, %[msk], v79\n\t" \
  "v_fmac_f32 %[qx], v112, v112\n\t" \
  "v_fmac_f32 %[qy], v113, v113\n\t" \
  "v_fmac_f32 %[qz], v114, v114\n\t" \
  "v_fmac_f32 %[qw], v115, v115\n\t" \
  "s_branch .Lhe%=\n\t" \
  ".Lhn%=:\n\t" \
  "ds_read_b128 v[64:67], %[a0]\n\t" \
  "ds_read_b128 v[68:71], %[a1]\n\t" \
  "ds_read_b128 v[72:75], %[a2]\n\t" \
  "ds_read_b128 v[76:79], %[a3]\n\t" \
  "s_waitcnt lgkmcnt(0)\n\t" \
  "v_lshlrev_b32 v112, 16, v64\n\t" \
  "v_and_b32 v113, %[msk], v64\n\t" \
  "v_lshlrev_b32 v114, 16, v65\n\t" \
  "v_and_b32 v115, %[msk], v65\n\t" \
  "v_fmac_f32 %[qx], v112, v112\n\t" \
  "v_fmac_f32 %[qy], v113, v113\n\t" \
  "v_fmac_f32 %[qz], v114, v114\n\t" \
  "v_fmac_f32 %[qw], v115, v115\n\t" \
  "v_lshlrev_b32 v112, 16, v66\n\t" \
  "v_and_b32 v113, %[msk], v66\n\t" \
  "v_lshlrev_b32 v114, 16, v67\n\t" \
  "v_and_b32 v115, %[msk], v67\n\t" \
  "v_fmac_f32 %[qx], v112, v112\n\t" \
  "v_fmac_f32 %[qy], v113, v113\n\t" \
  "v_fmac_f32 %[qz], v114, v114\n\t" \
  "v_fmac_f32 %[qw], v115, v115\n\t" \
  "v_lshlrev_b32 v112, 16, v68\n\t" \
  "v_and_b32 v113, %[msk], v68\n\t" \
  "v_lshlrev_b32 v114, 16, v69\n\t" \
  "v_and_b32 v115, %[msk], v69\n\t" \
  "v_fmac_f32 %[qx], v112, v112\n\t" \
  "v_fmac_f32 %[qy], v113, v113\n\t" \
  "v_fmac_f32 %[qz], v114, v114\n\t" \
  "v_fmac_f32 %[qw], v115, v115\n\t" \
  "v_lshlrev_b32 v112, 16, v70\n\t" \
  "v_and_b32 v113, %[msk], v70\n\t" \
  "v_lshlrev_b32 v114, 16, v71\n\t" \
  "v_and_b32 v115, %[msk], v71\n\t" \
  "v_fmac_f32 %[qx], v112, v112\n\t" \
  "v_fmac_f32 %[qy], v113, v113\n\t" \
  "v_fmac_f32 %[qz], v114, v114\n\t" \
  "v_fmac_f32 %[qw], v115, v115\n\t" \
  "s_waitcnt lgkmcnt(0)\n\t" \
  "s_setprio 3\n\t" \
  "v_mfma_f32_16x16x32_bf16 %[sr], %[sel], v[64:67], %[sr]\n\t" \
  "v_mfma_f32_16x16x32_bf16 %[sr], %[sel], v[68:71], %[sr]\n\t" \
  "v_mfma_f32_16x16x32_bf16 %[sr], %[sel], v[72:75], %[sr]\n\t" \
  "v_mfma_f32_16x16x32_bf16 %[sr], %[sel], v[76:79], %[sr]\n\t" \
  "s_setprio 2\n\t" \
  "v_lshlrev_b32 v112, 16, v72\n\t" \
  "v_and_b32 v113, %[msk], v72\n\t" \
  "v_lshlrev_b32 v114, 16, v73\n\t" \
  "v_and_b32 v115, %[msk], v73\n\t" \
  "v_fmac_f32 %[qx], v112, v112\n\t" \
  "v_fmac_f32 %[qy], v113, v113\n\t" \
  "v_fmac_f32 %[qz], v114, v114\n\t" \
  "v_fmac_f32 %[qw], v115, v115\n\t" \
  "v_lshlrev_b32 v112, 16, v74\n\t" \
  "v_and_b32 v113, %[msk], v74\n\t" \
  "v_lshlrev_b32 v114, 16, v75\n\t" \
  "v_and_b32 v115, %[msk], v75\n\t" \
  "v_fmac_f32 %[qx], v112, v112\n\t" \
  "v_fmac_f32 %[qy], v113, v113\n\t" \
  "v_fmac_f32 %[qz], v114, v114\n\t" \
  "v_fmac_f32 %[qw], v115, v115\n\t" \
  "v_lshlrev_b32 v112, 16, v76\n\t" \
  "v_and_b32 v113, %[msk], v76\n\t" \
  "v_lshlrev_b32 v114, 16, v77\n\t" \
  "v_and_b32 v115, %[msk], v77\n\t" \
  "v_fmac_f32 %[qx], v112, v112\n\t" \
  "v_fmac_f32 %[qy], v113, v113\n\t" \
  "v_fmac_f32 %[qz], v114, v114\n\t" \
  "v_fmac_f32 %[qw], v115, v115\n\t" \
  "v_lshlrev_b32 v112, 16, v78\n\t" \
  "v_and_b32 v113, %[msk], v78\n\t" \
  "v_lshlrev_b32 v114, 16, v79\n\t" \
  "v_and_b32 v115, %[msk], v79\n\t" \
  "v_fmac_f32 %[qx], v112, v112\n\t" \
  "v_fmac_f32 %[qy], v113, v113\n\t" \
  "v_fmac_f32 %[qz], v114, v114\n\t" \
  "v_fmac_f32 %[qw], v115, v115\n\t" \
  ".Lhe%=:\n\t" \
  "s_mov_b32 %[d], 40960\n\t" \
  "s_add_u32 %[sl], %[sl], 1\n\t" \
  "s_cmp_eq_u32 %[sl], 3\n\t" \
  "s_cselect_b32 %[d], 0xfffec000, %[d]\n\t" \
  "s_cselect_b32 %[sl], 0, %[sl]\n\t" \
  "v_add_u32 %[a0], %[d], %[a0]\n\t" \
  "v_add_u32 %[a1], %[d], %[a1]\n\t" \
  "v_add_u32 %[a2], %[d], %[a2]\n\t" \
  "v_add_u32 %[a3], %[d], %[a3]\n\t" \
  "v_add_u32 %[tb], %[d], %[tb]\n\t" \
  "v_add_u32 %[tc], %[d], %[tc]\n\t" \
  "s_add_u32 %[i], %[i], 1\n\t" \
  "s_cmp_lt_u32 %[i], 8\n\t" \
  "s_cbranch_scc1 .Lh%=\n\t" \
  "s_nop 7\n\t" \
  ""
constexpr int K1_SLOT = 40960, K1_SIDE = 3 * K1_SLOT, K1_MISC = K1_SIDE + 12288, K1_LDS = K1_MISC + 2048;
constexpr float QSCALE = 0.25f * 1.4426950408889634f;
#define K1HP 2
#define PF_CODE 1
#define PF_STEP 0x1800
#define K1LP 0
#define K1CP 1
#define K1ROT (2 * (xcd & 3) + (xcd & 4))
#define K1ST(base, off, val) asm volatile("global_store_dwordx4 %0, %1, %2 sc1\n\ts_nop 1" :: "v"(off), "v"(val), "s"(base) : "memory")

template <int NU>
DI void k1_stream(const char* xp, const char* wp, unsigned pitch, unsigned ustr, int rot, int wid, int lane, unsigned lds0) {
  const int r8 = lane >> 3, p8 = lane & 7;
  const unsigned xe = (wid * 32 + r8) * 1024 + ((p8 ^ (r8 >> 1)) << 4), xo = (wid * 32 + r8) * 1024 + ((p8 ^ ((r8 >> 1) + 4)) << 4);
  const unsigned x0 = xe, x1 = xo + 7168, x2 = xe + 14336, x3 = xo + 21504;
  const unsigned wv = (wid * 16 + r8) * pitch + ((p8 ^ ((r8 & 2) << 1)) << 4) + 4096;
  const unsigned w0 = wv + 2048, w1 = wv + 8 * pitch + 1024, w2 = wv + ustr, w3 = wv + 8 * pitch + ustr - 1024, w4 = wv + 2 * ustr - 2048, w5 = wv + 8 * pitch + 2 * ustr - 3072;
  auto stage = [&](int i, int sl) LAM {
    const int s = (i + rot) & 7;
    const char* xs = xp + s * 128; const char* wsrc = wp + SOFF(s * 64 * pitch);
    const unsigned ma = lds0 + sl * K1_SLOT + wid * 4096, mb = lds0 + sl * K1_SLOT + 16384 + wid * 6144 + 2048;
    asm volatile("s_mov_b32 m0, %10\n\ts_nop 0\n\t"
                 "global_load_lds_dwordx4 %0, %8\n\tglobal_load_lds_dwordx4 %1, %8 offset:1024\n\tglobal_load_lds_dwordx4 %2, %8 offset:2048\n\tglobal_load_lds_dwordx4 %3, %8 offset:3072\n\t"
                 "s_mov_b32 m0, %11\n\ts_nop 0\n\t"
                 "global_load_lds_dwordx4 %4, %9 offset:-2048\n\tglobal_load_lds_dwordx4 %5, %9 offset:-1024\n\tglobal_load_lds_dwordx4 %6, %9\n\tglobal_load_lds_dwordx4 %7, %9 offset:1024"
                 :: "v"(x0), "v"(x1), "v"(x2), "v"(x3), "v"(w0), "v"(w1), "v"(w2), "v"(w3), "s"(xs), "s"(wsrc), "s"(ma), "s"(mb) : "memory");
    if (NU == 3) asm volatile("global_load_lds_dwordx4 %0, %2 offset:2048\n\tglobal_load_lds_dwordx4 %1, %2 offset:3072" :: "v"(w4), "v"(w5), "s"(wsrc) : "memory");
  };
  stage(0, 0); stage(1, 1);
  int sl = 2;
#pragma unroll 1
  for (int i = 0; i < 6; i++) { wait_bar<4 + 2 * NU>(); stage(i + 2, sl); sl = sl == 2 ? 0 : sl + 1; }
  wait_bar<4 + 2 * NU>();
}

__global__ __launch_bounds__(1024, 4) void k1_kernel(const char* x, const char* wqkv, const char* wo, const char* w_in, const char* w_out, char* ws) {
  unsigned long long pc0; asm volatile("s_getpc_b64 %0" : "=s"(pc0)); pc0 &= ~255ull;
  extern __shared__ __attribute__((aligned(16))) char lds[];
  const int wid = __builtin_amdgcn_readfirstlane(TIDX() >> 6);
  const int b = BIDX();
  const int xcd = b & 7, j = b >> 3;
  const int panel = (xcd & 3) * 4 + (j & 3), gi = j >> 2, grp = (xcd >> 2) * 4 + (gi & 3);
  const bool isz = gi >= 4, spare = isz && grp == 7;
  const int rot = K1ROT;
  const unsigned lds0 = (unsigned)(uintptr_t)lds;
  const LAS char* L = (const LAS char*)lds;
  LAS char* LW = (LAS char*)lds;
  if (__builtin_expect(wid < 4, 1)) {
    const int lane = lane_id(), h2 = lane >> 5;
    __builtin_amdgcn_s_setprio(K1LP);
    const char* xp = x + (size_t)panel * 131072;
    if (!isz) k1_stream<3>(xp, wqkv + grp * 128 - 4096, 3072, 1024, rot, wid, lane, lds0);
    else if (!spare) k1_stream<2>(xp, w_in + 1792 + grp * 256 - 4096, 3584, 128, rot, wid, lane, lds0);
    int sid[3];
#pragma unroll
    for (int i = 0; i < 3; i++) {
      const int t = wid * 3 + i; int id = b * 9 + t; if (t >= 9) id = -1;
      sid[i] = id; if (id < 0) id = 0;
      const char* src; unsigned pitch;
      if (id < 896) { src = w_in + SOFF((id / 28) * 16 * 3584 + (id % 28) * 64); pitch = 3584; }
      else { const int u = id - 896, ks = u >> 4; src = (ks < 32 ? wo + (size_t)ks * 16384 : w_out + (size_t)(ks - 32) * 16384) + (u & 15) * 64; pitch = 1024; }
      const unsigned vo = (lane >> 2) * pitch + (lane & 3) * 16, mb = lds0 + K1_SIDE + t * 1024;
      asm volatile("s_mov_b32 m0, %2\n\ts_nop 0\n\tglobal_load_lds_dwordx4 %0, %1" :: "v"(vo), "s"(src), "s"(mb) : "memory");
    }
    if (!spare) wait_bar<3>();
    if (wid == 3 && b < 32) { u32x4 z = {0u, 0u, 0u, 0u}; K1ST(ws + WS_FL + b * 1024, (unsigned)lane * 16, z); }
    if (!spare) raw_bar();
    asm volatile("s_waitcnt vmcnt(0)" ::: "memory");
    const int trb = ((lane & 15) >> 2) * 64 + ((lane >> 4) & 1) * 32 + (lane & 3) * 8;
#pragma unroll
    for (int i = 0; i < 3; i++) {
      const int id = sid[i];
      const bool nat = id < 896;
      const u32x4 f = trfrag(L + K1_SIDE + (wid * 3 + i) * 1024 + trb + h2 * (nat ? 512 : 256), 0, nat ? 256 : 512);
      if (id >= 0) {
        char* dst = ws + SOFF(nat ? WS_WIN + ((id % 28) * 32 + id / 28) * 1024 : WS_WC + (((id - 896) & 15) * 88 + ((id - 896) >> 4)) * 1024);
        K1ST(dst, (unsigned)lane * 16, f);
      }
    }
    return;
  }
  if (j >= 28 && wid >= 12) {
    const unsigned l = lane_id(); unsigned t;
    asm volatile("global_load_dword %0, %1, %2\n\ts_waitcnt vmcnt(0)" : "=&v"(t) : "v"(l * 128 + (wid - 12) * PF_STEP), "s"(pc0) : "memory");
  }
  if (spare) return;
  constexpr int MU = K1_MISC, RS = MU + 512, CSF = RS + 512;
  const int m = wid & 3, rt = panel * 4 + m;
#define K1_LANE const int lane = lane_id(), r32 = lane & 31, h2 = lane >> 5, arow = m * 32 + r32, asw = (arow >> 1) & 7, q4 = (lane & 15) >> 2, \
    trw = 16384 + (8 * h2 + q4) * 128 + ((lane >> 4) & 1) * 32 + (lane & 3) * 8
  if (wid < 8) {
    K1_LANE; const unsigned l16 = lane * 16;
    const unsigned selw = (lane & 15) == ((lane >> 4) & 1) ? 0x3f803f80u : 0u;
    const u32x4 sel = {selw, selw, selw, selw};
    f32x4 sr = {}, s0 = {}, s1 = {};
    float qx = 0.f, qy = 0.f, qz = 0.f, qw = 0.f;
    u32x4 xk[4];
    unsigned aa[4], tb = lds0 + trw + (m < 3 - (int)isz ? m : 0) * 2048 + ((q4 >> 1) << 6), tc = tb ^ 64;
#pragma unroll
    for (int ks = 0; ks < 4; ks++) aa[ks] = lds0 + arow * 128 + (((2 * ks + h2) ^ asw) << 4);
    __builtin_amdgcn_s_setprio(K1HP);
    { unsigned ti, tsl, td;
      asm volatile(K1_HLOOP : [sr] "+v"(sr), [s0] "+v"(s0), [s1] "+v"(s1), [qx] "+v"(qx), [qy] "+v"(qy), [qz] "+v"(qz), [qw] "+v"(qw),
                   [a0] "+v"(aa[0]), [a1] "+v"(aa[1]), [a2] "+v"(aa[2]), [a3] "+v"(aa[3]), [tb] "+v"(tb), [tc] "+v"(tc),
                   [x0] "=&v"(xk[0]), [x1] "=&v"(xk[1]), [x2] "=&v"(xk[2]), [x3] "=&v"(xk[3]), [i] "=&s"(ti), [sl] "=&s"(tsl), [d] "=&s"(td)
                   : [sel] "v"(sel), [keep] "s"((grp - rot) & 7), [cs] "s"(m + (gi >> 2)), [msk] "s"(0xffff0000u) : "memory", "scc", "v60", "v61", "v62", "v63", ATTN_CLOBBERS);
    }
    qx = xhalf_add((qx + qy) + (qz + qw));
    if (lane < 16) { LAS float* cp = (LAS float*)(LW + CSF + m * 256) + lane; cp[0] = s0[0]; cp[16] = s0[1]; cp[32] = s1[0]; cp[48] = s1[1];
      LAS float* mp = (LAS float*)(LW + MU + m * 128) + lane; mp[0] = sr[0] * (1.f / 512.f); mp[16] = sr[1] * (1.f / 512.f); }
    const float mu = *(const LAS float*)(L + MU + arow * 4), rs = __builtin_amdgcn_rsqf(__builtin_fmaxf(qx * (1.f / 512.f) - mu * mu, 0.f) + 1e-5f);
    *(LAS float*)(LW + RS + arow * 4) = rs;
    lds_bar();
    if (!isz) {
      const float nm = -mu * rs;
      char* dx = ws + SOFF(WS_XN + (rt * 32 + 4 * grp) * 1024);
#pragma unroll
      for (int ks = 0; ks < 4; ks++) { u32x4 y;
#pragma unroll
        for (int e = 0; e < 4; e++) y[e] = cvtpk(__builtin_fmaf(bflo(xk[ks][e]), rs, nm), __builtin_fmaf(bfhi(xk[ks][e]), rs, nm));
        K1ST(dx + ks * 1024, l16, y); }
    }
    return;
  }
  const int nh = (wid >> 2) & 1;
  K1_LANE;
  unsigned aa[4], ba = lds0 + trw + ((nh ^ (q4 >> 1)) << 6);
#pragma unroll
  for (int ks = 0; ks < 4; ks++) aa[ks] = lds0 + arow * 128 + (((2 * ks + h2) ^ asw) << 4);
  f32x16 c0 = {}, c1 = {}, c2 = {};
  PINV(c0); PINV(c1); PINV(c2);
  __builtin_amdgcn_s_setprio(K1CP);
  raw_bar();
  { unsigned ti, tsl, td;
    asm volatile(K1_LOOP : [c0] "+v"(c0), [c1] "+v"(c1), [c2] "+v"(c2), [a0] "+v"(aa[0]), [a1] "+v"(aa[1]), [a2] "+v"(aa[2]), [a3] "+v"(aa[3]), [b] "+v"(ba),
                 [i] "=&s"(ti), [sl] "=&s"(tsl), [d] "=&s"(td) : [z] "s"(gi >> 2) : "memory", "scc", ATTN_CLOBBERS);
  }
  const int lane_ = lane_id();
  const int r32_ = lane_ & 31, h2_ = lane_ >> 5, arow_ = m * 32 + r32_; const unsigned l16_ = lane_ * 16;
  const float mu = *(const LAS float*)(L + MU + arow_ * 4), rs = *(const LAS float*)(L + RS + arow_ * 4), csv = *(const LAS float*)(L + CSF + 512 + nh * 128 + r32_ * 4);
  const LAS char* pc = L + CSF + nh * 128 + h2_ * 16;
  const LAS char* pm = L + MU + m * 128 + h2_ * 16;
#pragma unroll
  for (int g = 0; g < 4; g++) { const f32x4 a = *(const LAS f32x4*)(pc + g * 32), c = *(const LAS f32x4*)(pc + 256 + g * 32);
#pragma unroll
    for (int e = 0; e < 4; e++) { c0[4 * g + e] = __builtin_fmaf(-mu, a[e], c0[4 * g + e]); c1[4 * g + e] = __builtin_fmaf(-mu, c[e], c1[4 * g + e]); } }
  if (!isz) {
    const int hd = grp * 4 + nh * 2;
    char* kvb = ws + SOFF(WS_KV + hd * 131072 + (rt >> 3) * 16384 + (rt & 7) * 1024);
    char* qb = ws + SOFF(WS_QF + hd * 65536 + rt * 1024);
    const float sq = rs * QSCALE;
    f32x4 mvv[4], rvv[4];
#pragma unroll
    for (int g = 0; g < 4; g++) { mvv[g] = *(const LAS f32x4*)(pm + g * 32); rvv[g] = *(const LAS f32x4*)(pm + 512 + g * 32); }
#pragma unroll
    for (int s = 0; s < 2; s++) {
      u32x4 o, k, v;
#pragma unroll
      for (int e = 0; e < 4; e++) { k[e] = cvtpk(c1[8 * s + 2 * e] * rs, c1[8 * s + 2 * e + 1] * rs); o[e] = cvtpk(c0[8 * s + 2 * e] * sq, c0[8 * s + 2 * e + 1] * sq); }
      K1ST(kvb + s * 131072, l16_, k);
#pragma unroll
      for (int g = 0; g < 2; g++) { const f32x4 mv = mvv[2 * s + g], rv = rvv[2 * s + g]; const int q = 8 * s + 4 * g;
        v[2 * g] = cvtpk(__builtin_fmaf(-mv[0], csv, c2[q]) * rv[0], __builtin_fmaf(-mv[1], csv, c2[q + 1]) * rv[1]);
        v[2 * g + 1] = cvtpk(__builtin_fmaf(-mv[2], csv, c2[q + 2]) * rv[2], __builtin_fmaf(-mv[3], csv, c2[q + 3]) * rv[3]); }
      K1ST(kvb + 8192 + s * 512, (unsigned)((r32_ >> 4) * 131072 + h2_ * 256 + (lane_ & 15) * 16), v);
      K1ST(qb + s * 65536, l16_, o);
    }
  } else {
    char* zr = ws + SOFF(WS_ZR + (rt * 56 + grp * 8 + nh * 2) * 1024);
    float s = 0.f, q = 0.f;
#pragma unroll
    for (int u = 0; u < 2; u++)
#pragma unroll
      for (int t = 0; t < 2; t++) {
        u32x4 o;
#pragma unroll
        for (int e = 0; e < 4; e++) { const int r = 8 * t + 2 * e; const float a = gelu_t((u ? c1[r] : c0[r]) * rs), c = gelu_t((u ? c1[r + 1] : c0[r + 1]) * rs);
          s += a; q = __builtin_fmaf(a, a, q); s += c; q = __builtin_fmaf(c, c, q); o[e] = cvtpk(a, c); }
        K1ST(zr + u * 4096 + t * 1024, l16_, o);
      }
    const f32x2 t = {xhalf_add(s), xhalf_add(q)};
    asm volatile("s_mov_b32 exec_hi, 0\n\tglobal_store_dwordx2 %0, %1, %2 sc1\n\ts_mov_b32 exec_hi, -1" :: "v"((unsigned)(r32_ * 16)), "v"(t), "s"(ws + SOFF(WS_ZS + rt * 4096 + grp * 512 + nh * 8)) : "memory");
  }
}
constexpr int K2_LDS = 81920;
constexpr int ATT_SLOT = 16384, ATT_ONES = 4 * ATT_SLOT;
constexpr unsigned FL_ZF = 0, FL_UF = FL_ZF + 8192, FL_OF = FL_UF + 8192;
constexpr unsigned SPIN_MAX = 1u << 18;
typedef __amdgpu_buffer_rsrc_t rsrc_t;
DI rsrc_t mkrsrc(const void* p) { return __builtin_amdgcn_make_buffer_rsrc((void*)p, 0, 0x7fffffff, 0x00020000); }
DI u32x4 ld16(rsrc_t r, unsigned vo, unsigned so) { return __builtin_amdgcn_raw_buffer_load_b128(r, vo, so, 0); }
DI unsigned ld4c(rsrc_t r, unsigned vo, unsigned so) { return __builtin_amdgcn_raw_buffer_load_b32(r, vo, so, 16); }
DI void st16c(u32x4 v, rsrc_t r, unsigned vo, unsigned so) { __builtin_amdgcn_raw_buffer_store_b128(v, r, vo, so, 16); }
DI void st4c(unsigned v, rsrc_t r, unsigned vo, unsigned so) { __builtin_amdgcn_raw_buffer_store_b32(v, r, vo, so, 16); }
DI unsigned nosent(unsigned v) { return v < 0xfffeffffu ? v : 0xfffeffffu; }
DI void vm_drain() { asm volatile("s_waitcnt vmcnt(0)" ::: "memory"); }

DI void attn_body(char* ws, int b, char* lds) {
  const int tid = TIDX(), lane = tid & 63; const int wid = __builtin_amdgcn_readfirstlane(tid >> 6);
  const int xcd = b & 7, j = b >> 3, h = xcd * 4 + (j >> 3), qt = (j & 7) * 8 + wid;
  __builtin_amdgcn_s_setprio(3);
  const unsigned lds0 = (unsigned)(uintptr_t)lds;
  const char* gb = ws + WS_KV + (size_t)h * 131072;
  unsigned goff = wid * 2048 + lane * 16;
  const unsigned mb = lds0 + wid * 2048;
  const rsrc_t afr = mkrsrc(ws + WS_AF);
  const unsigned oso = SOFF((qt * 88 + h) * 1024);
  u32x4 qf;
  { const char* qp = ws + WS_QF + ((size_t)h * 64 + qt) * 1024; const unsigned lo = lane * 16;
    asm volatile("global_load_dwordx4 %0, %1, %2" : "=v"(qf) : "v"(lo), "s"(qp) : "memory"); }
  glds2(gb, goff, mb);
  glds2(gb, goff + 16384, mb + ATT_SLOT);
  goff += 32768;
  { const unsigned ov = (tid & 11) == 0 ? 0x3f803f80u : 0u; u32x4 ones = {ov, ov, ov, ov};
    *(LAS u32x4*)((LAS char*)lds + ATT_ONES + tid * 16) = ones; }
  const bool isd = (lane & 16) == 0;
  const unsigned kl = lds0 + lane * 16;
  const unsigned vb = lds0 + (isd ? 8192 + (lane >> 5) * 256 + (lane & 15) * 16 : ATT_ONES + (lane & 15) * 16);
  const unsigned dm = isd ? 0xffffffffu : 0u;
  asm volatile("s_waitcnt vmcnt(2) lgkmcnt(0)\n\ts_barrier" : "+v"(qf) :: "memory");
  f32x16 negm, o = {};
  {
    const LAS char* kp = (const LAS char*)lds + lane * 16;
    const f32x16 z = {};
    const f32x16 s0 = MFMA32(*(const LAS u32x4*)kp, qf, z), s1 = MFMA32(*(const LAS u32x4*)(kp + 1024), qf, z);
    float m = __builtin_fmaxf(s0[0], s1[0]);
#pragma unroll
    for (int r = 1; r < 16; r++) m = __builtin_fmaxf(m, __builtin_fmaxf(s0[r], s1[r]));
    auto rr = __builtin_amdgcn_permlane32_swap(F2U(m), F2U(m), false, false);
    m = -(__builtin_fmaxf(U2F(rr[0]), U2F(rr[1])) + ATT_OFS);
#pragma unroll
    for (int r = 0; r < 16; r++) negm[r] = m;
  }
  { unsigned c, sc, t0, sm0; const u32x4 pre = {~0u, ~0u, ~0u, ~0u};
    asm volatile(ATTN_LOOP_ASM
      : [o] "+v"(o), [goff] "+v"(goff), [c] "=&s"(c), [sc] "=&s"(sc), [t0] "=&s"(t0), [sm0] "=&s"(sm0)
      : [q] "v"(qf), [negm] "v"(negm), [kl] "v"(kl), [vb] "v"(vb), [dm] "v"(dm), [gb] "s"(gb), [mb] "s"(mb), [op] "s"(ws + WS_AF + oso), [hp] "s"(ws + WS_FL + FL_OF + qt * 128 + h * 4), [pre] "v"(pre), [l16] "v"(lane * 16)
      : "memory", "scc", ATTN_CLOBBERS); }
  {
    float chk = 0.f;
#pragma unroll
    for (int r = 0; r < 8; r++) chk += __builtin_fabsf(o[r]);
    const bool bad = !(o[8] <= 0x1p100f && o[8] >= 0x1p-110f && chk <= 3.0e38f && chk >= 0x1p-112f);
    if (__builtin_expect(WANY(bad), 0)) {
      const rsrc_t kv = mkrsrc(gb);
      const f32x16 z = {};
      float m = -3.0e38f;
#pragma unroll 1
      for (int t = 0; t < 64; t++) {
        const f32x16 s = MFMA32(ld16(kv, lane * 16, (t >> 3) * 16384 + (t & 7) * 1024), qf, z);
#pragma unroll
        for (int r = 0; r < 16; r++) m = __builtin_fmaxf(m, s[r]);
      }
      auto rr = __builtin_amdgcn_permlane32_swap(F2U(m), F2U(m), false, false);
      m = __builtin_fmaxf(U2F(rr[0]), U2F(rr[1]));
      o = z;
      const unsigned pad = (lane & 11) == 0 ? 0x3f803f80u : 0u;
#pragma unroll 1
      for (int t = 0; t < 64; t++) {
        const unsigned so = (t >> 3) * 16384 + (t & 7) * 1024;
        const f32x16 s = MFMA32(ld16(kv, lane * 16, so), qf, z);
#pragma unroll
        for (int u = 0; u < 2; u++) {
          u32x4 v = ld16(kv, 8192 + u * 512 + (lane >> 5) * 256 + (lane & 15) * 16, so), p;
#pragma unroll
          for (int r = 0; r < 4; r++) { v[r] = isd ? v[r] : pad; p[r] = cvtpk(__builtin_amdgcn_exp2f(s[8 * u + 2 * r] - m), __builtin_amdgcn_exp2f(s[8 * u + 2 * r + 1] - m)); }
          o = MFMA32(v, p, o);
        }
      }
    }
  }
  const float rl = __builtin_amdgcn_rcpf(o[8]);
  u32x4 ov;
#pragma unroll
  for (int r = 0; r < 4; r++) ov[r] = nosent(cvtpk(o[2 * r] * rl, o[2 * r + 1] * rl));
  st16c(ov, afr, lane * 16, oso);
}

DI void gemm_body(const char* x, const char* w_sp, const char* b_sp, char* out, char* ws, int gb, char* lds) {
  const int tid = TIDX(), lane = tid & 63; const int wid = __builtin_amdgcn_readfirstlane(tid >> 6);
  const int xcd = gb & 7, j = gb >> 3;
  const int p = (xcd >> 1) * 8 + (j & 7), ot = (xcd & 1) * 4 + (j >> 3);
  const int nb = ot;
  const int r32 = lane & 31, h2 = lane >> 5;
  const unsigned lds0 = (unsigned)(uintptr_t)lds, l16 = lane * 16;
  const LAS char* L = (const LAS char*)lds;
  LAS char* LW = (LAS char*)lds;
  const char* flp = ws + WS_FL;
  const rsrc_t fl = mkrsrc(flp);
  unsigned t0;
  u32x2 x0, x1;
  const unsigned xso = SOFF((p * 2 + (wid & 1)) * 32768 + (ot * 64 + (wid >> 1) * 16) * 2);
  const char* wc = ws + WS_WC;
  const int fw = (ot * 2 + (wid >> 2)) * 88;
  const unsigned mb2 = lds0 + wid * 2048;
  const char* wf = wc + SOFF((fw + 34 + (wid & 3) * 14) * 1024);
  if (nb < 7) {
    const int nq = wid & 3, kh = wid >> 2, cb = nb * 4 + nq, rt = p * 2 + kh;
    constexpr int SCR = 65536, EXG = 49152;
    const int rot = SOFF((4 * nb + (p >> 3)) & 15);
    const unsigned long long wq = (unsigned long long)(w_sp + (size_t)p * 64 * 4096);
    const unsigned wl = (unsigned)wq, wh = (unsigned)(wq >> 32);
    unsigned av0, av1;
    { const int row = wid * 8 + (lane >> 4); av0 = row * 4096 + (((lane & 15) ^ (row & 15)) << 4); av1 = (row + 4) * 4096 + (((lane & 15) ^ ((row + 4) & 15)) << 4) - 1024; }
    const char* zb = ws + WS_Z2 + (size_t)cb * 131072;
    __builtin_amdgcn_s_setprio(GPRIO_Z);
    { const rsrc_t zr = mkrsrc(zb);
      u32x2 t[7]; u32x4 f[2];
      asm volatile("s_nop 4\n\tglobal_load_dwordx2 %0, %10, %11\n\tglobal_load_dwordx2 %1, %10, %11 offset:512\n\tglobal_load_dwordx2 %2, %10, %11 offset:1024\n\tglobal_load_dwordx2 %3, %10, %11 offset:1536\n\t"
                   "global_load_dwordx2 %4, %10, %11 offset:2048\n\tglobal_load_dwordx2 %5, %10, %11 offset:2560\n\tglobal_load_dwordx2 %6, %10, %11 offset:3072\n\t"
                   "global_load_dwordx4 %7, %9, %12\n\tglobal_load_dwordx4 %8, %9, %12 offset:1024\n\ts_mov_b32 m0, %14\n\ts_barrier\n\t"
                   "global_load_lds_dwordx4 %9, %13 offset:-4096\n\tglobal_load_lds_dwordx4 %9, %13 offset:-3072\n\tglobal_load_lds_dwordx4 %9, %13 offset:-2048\n\tglobal_load_lds_dwordx4 %9, %13 offset:-1024\n\t"
                   "global_load_lds_dwordx4 %9, %13\n\tglobal_load_lds_dwordx4 %9, %13 offset:1024\n\tglobal_load_lds_dwordx4 %9, %13 offset:2048\n\tglobal_load_lds_dwordx4 %9, %13 offset:3072\n\t"
                   "s_waitcnt vmcnt(8)"
                   : "=&v"(t[0]), "=&v"(t[1]), "=&v"(t[2]), "=&v"(t[3]), "=&v"(t[4]), "=&v"(t[5]), "=&v"(t[6]), "=&v"(f[0]), "=&v"(f[1])
                   : "v"(l16), "v"(r32 * 16 + h2 * 8), "s"(ws + WS_ZS + rt * 4096), "s"(ws + WS_ZR + SOFF((rt * 56 + cb * 2) * 1024)),
                     "s"(ws + WS_XN + (size_t)p * 65536 + wid * 8192 + 4096), "s"(lds0 + (wid & 1) * 32768 + (wid >> 1) * 8192 + 4096) : "memory");
      float s = 0.f, q = 0.f;
#pragma unroll
      for (int w = 0; w < 7; w++) { s += U2F(t[w][0]); q += U2F(t[w][1]); }
      s = xhalf_add(s); q = xhalf_add(q);
      const float mu = s * (1.f / 896.f);
      const float rs = __builtin_amdgcn_rsqf(__builtin_fmaxf(q * (1.f / 896.f) - mu * mu, 0.f) + 1e-5f), nm = -mu * rs;
      LAS char* tw = LW + SCR + wid * 2048 + r32 * 64 + h2 * 8;
      const int q4 = (lane & 15) >> 2, pp = lane & 3, ch = ((lane >> 4) & 1) * 2 + (pp >> 1);
#pragma unroll
      for (int g = 0; g < 4; g++) {
        const unsigned a = f[g >> 1][(g & 1) * 2], c = f[g >> 1][(g & 1) * 2 + 1];
        const u32x2 v = {cvtpk(__builtin_fmaf(bflo(a), rs, nm), __builtin_fmaf(bfhi(a), rs, nm)), cvtpk(__builtin_fmaf(bflo(c), rs, nm), __builtin_fmaf(bfhi(c), rs, nm))};
        *(LAS u32x2*)(tw + ((g ^ ((r32 >> 1) & 3)) << 4)) = v;
      }
#pragma unroll
      for (int ks = 0; ks < 2; ks++) {
        const int s0 = 16 * ks + 8 * h2 + q4, s1 = s0 + 4;
        const u32x2 lo = trread(L + SCR + wid * 2048 + s0 * 64 + ((ch ^ ((s0 >> 1) & 3)) << 4) + (pp & 1) * 8);
        const u32x2 hi = trread(L + SCR + wid * 2048 + s1 * 64 + ((ch ^ ((s1 >> 1) & 3)) << 4) + (pp & 1) * 8);
        const u32x4 o = {lo[0], lo[1], hi[0], hi[1]};
        st16c(o, zr, l16, (rt * 2 + ks) * 1024);
      }
    }
    const rsrc_t af = mkrsrc(ws + WS_AF);
    const unsigned uso = SOFF((rt * 88 + 32 + cb * 2) * 1024);
    unsigned zp[8];
    { f32x16 z0, z1;
      asm volatile(G_Z_ASM : [z0] "=&v"(z0), [z1] "=&v"(z1), [t0] "=&s"(t0)
                   : [l16] "v"(l16), [la] "v"(lds0 + kh * 24576 + l16), [lc] "v"(lds0 + 16384 - kh * 8192 + l16), [av0] "v"(av0), [av1] "v"(av1),
                     [wp] "s"(ws + WS_WIN + 4096 + SOFF(cb * 32768 + kh * 16384)),
                     [zf] "s"(flp + SOFF(FL_ZF + nb * 128 + p * 4)), [wid] "s"(wid),
                     [mb] "s"(mb2), [rot] "s"(rot), [wl] "s"(wl), [wh] "s"(wh)
                   : "memory", "scc", G_CLOB48, ATTN_CLOBBERS, G_SCLOB);
      __builtin_amdgcn_s_setprio(GPRIO_V);
#pragma unroll
      for (int g = 0; g < 4; g++) { const f32x4 c = {z1[4 * g], z1[4 * g + 1], z1[4 * g + 2], z1[4 * g + 3]}; *(LAS f32x4*)(LW + EXG + wid * 4096 + g * 1024 + l16) = c; }
      lds_bar();
#pragma unroll
      for (int g = 0; g < 4; g++) { const f32x4 t = *(const LAS f32x4*)(L + EXG + (wid ^ 4) * 4096 + g * 1024 + l16);
        zp[2 * g] = cvtpk(gelu_t(z0[4 * g] + t[0]), gelu_t(z0[4 * g + 1] + t[1])); zp[2 * g + 1] = cvtpk(gelu_t(z0[4 * g + 2] + t[2]), gelu_t(z0[4 * g + 3] + t[3])); }
#pragma unroll
      for (int g = 0; g < 8; g++) PINV(zp[g]);
    }
    f32x16 g0, g1; unsigned pfd, bbr;
    { unsigned ao[4];
#pragma unroll
      for (int k = 0; k < 4; k++) ao[k] = lds0 + kh * 8192 + r32 * 256 + (((8 * kh + 2 * k + h2) ^ (r32 & 15)) << 4);
      const unsigned long long zq = (unsigned long long)(zb + kh * 4096);
      asm volatile(G_S_ASM : [g0] "=&v"(g0), [g1] "=&v"(g1), [t0] "=&s"(t0), [pf] "=&v"(pfd), [bb] "=&v"(bbr), [x0] "=&v"(x0), [x1] "=&v"(x1)
                   : [l16] "v"(l16), [av0] "v"(av0), [av1] "v"(av1), [a0] "v"(ao[0]), [a1] "v"(ao[1]), [a2] "v"(ao[2]), [a3] "v"(ao[3]),
                     [c0] "v"(ao[0] ^ 8192), [c1] "v"(ao[1] ^ 8192), [c2] "v"(ao[2] ^ 8192), [c3] "v"(ao[3] ^ 8192),
                     [wid] "s"(wid), [fl] "s"(flp + FL_ZF + nb * 128), [rot] "s"(rot), [mb] "s"(mb2),
                     [wl] "s"(wl), [wh] "s"(wh), [zl] "s"((unsigned)zq), [zh] "s"((unsigned)(zq >> 32)), [up] "s"(flp + FL_UF + p * 128 + 28),
                     [xp] "s"(x + xso), [bp] "s"(b_sp + rt * 64), [wf] "s"(wf)
                   : "memory", "scc", "vcc", ATTN_CLOBBERS, G_SCLOB);
    }
    for (unsigned it = 0; it < SPIN_MAX && WANY(pfd == 0u); it++) { __builtin_amdgcn_s_sleep(4); pfd = ld4c(fl, 0, FL_UF + p * 128 + 28); }
    const float bq = U2F(bbr << 16);
    __builtin_amdgcn_s_setprio(GPRIO_V);
#pragma unroll
    for (int g = 0; g < 4; g++) { const f32x4 c = {g1[4 * g], g1[4 * g + 1], g1[4 * g + 2], g1[4 * g + 3]}; *(LAS f32x4*)(LW + EXG + wid * 4096 + g * 1024 + l16) = c; }
    lds_bar();
    {
#pragma unroll
      for (int s = 0; s < 2; s++) {
        u32x4 u;
#pragma unroll
        for (int g = 0; g < 2; g++) {
          const int o = 8 * s + 4 * g;
          const f32x4 t = *(const LAS f32x4*)(L + EXG + (wid ^ 4) * 4096 + (2 * s + g) * 1024 + l16);
          const unsigned za = zp[4 * s + 2 * g], zc = zp[4 * s + 2 * g + 1];
          u[2 * g] = nosent(cvtpk((g0[o] + t[0] + bq) * bflo(za), (g0[o + 1] + t[1] + bq) * bfhi(za)));
          u[2 * g + 1] = nosent(cvtpk((g0[o + 2] + t[2] + bq) * bflo(zc), (g0[o + 3] + t[3] + bq) * bfhi(zc)));
        }
        st16c(u, af, l16 + s * 1024, uso);
      }
    }
  } else {
    for (int i = 0; i < PF_DLY; i++) __builtin_amdgcn_s_sleep(64);
    const rsrc_t af = mkrsrc(ws + WS_AF + SOFF(((p * 2 + (wid & 1)) * 88 + 32 + (wid >> 1) * 14) * 1024));
    const u32x4 pre = {~0u, ~0u, ~0u, ~0u};
#pragma unroll
    for (int i = 0; i < 14; i++) st16c(pre, af, l16, i * 1024);
    vm_drain();
    raw_bar();
    if (tid == 0) st4c(1u, fl, 0, FL_UF + p * 128 + 28);
    x0 = *(const u32x2*)(x + xso + r32 * 1024 + h2 * 8); x1 = *(const u32x2*)(x + xso + r32 * 1024 + h2 * 8 + 16); PINV(x0); PINV(x1);
    asm volatile(G_F_ASM :: [l16] "v"(l16), [mb] "s"(mb2), [wf] "s"(wf) : "memory", "scc");
  }
  { const int mt = wid & 1, kq = wid >> 1, nt = kq >> 1, hf = kq & 1, rt = p * 2 + mt;
    const unsigned lb = (lane ^ (hf << 4)) * 16;
    const char* ap = ws + WS_AF + 4096 + SOFF(rt * 90112);
    const int f0 = (ot * 2 + nt) * 88, f1 = (ot * 2 + (nt ^ 1)) * 88;
    f32x16 c0 = {bflo(x0[0]), bfhi(x0[0]), bflo(x0[1]), bfhi(x0[1]), bflo(x1[0]), bfhi(x1[0]), bflo(x1[1]), bfhi(x1[1]), 0.f, 0.f, 0.f, 0.f, 0.f, 0.f, 0.f, 0.f}, c1;
    unsigned tb0, tb1, tv;
    __builtin_amdgcn_s_setprio(GPRIO_V);
    asm volatile(G_U_ASM : [c0] "+v"(c0), [c1] "=&v"(c1), [t0] "=&s"(t0), [b0] "=&v"(tb0), [b1] "=&v"(tb1), [tv] "=&v"(tv)
                 : [l16] "v"(l16), [lb] "v"(lb), [wid] "s"(wid),
                   [fu] "s"(flp + FL_UF + p * 128), [fq] "s"(flp + FL_OF + p * 256),
                   [m4] "s"(lds0 + 49152 + wid * 4096),
                   [w2] "s"(wf + 6144), [mb] "s"(mb2), [uf] "s"(flp + FL_UF + p * 128 + nb * 4),
                   [o0] "s"(wc + SOFF((f0 + 4 + 8 * kq) * 1024)), [o1] "s"(wc + SOFF((f1 + 4 + 8 * kq) * 1024)),
                   [wg] "s"(wc + SOFF((fw + 44 + (wid & 3) * 14) * 1024)), [wh] "s"(wc + SOFF((fw + 4 + (wid & 3) * 8) * 1024)),
                   [ap] "s"(ap + SOFF((32 + 14 * kq) * 1024)), [aq] "s"(ap + 8 * kq * 1024),
                   [sb0] "s"(lds0 + (nt * 4 + kq) * 2048), [se] "s"(lds0 + 49152 + nt * 16384 + kq * 4096)
                 : "memory", "scc", "vcc", G_CLOB48, ATTN_CLOBBERS, G_SCLOB);
    const unsigned orow = (rt * 32 + r32) * 1024 + (ot * 64 + kq * 16) * 2;
#pragma unroll
    for (int d = 1; d < 4; d++) {
      const f32x16& c = (d & 2) ? c1 : c0; const int o = (d & 1) * 8;
      LAS char* ex = LW + (wid ^ (2 * d)) * 6144 + (d - 1) * 2048 + l16;
      const f32x4 a = {c[o], c[o + 1], c[o + 2], c[o + 3]}, e = {c[o + 4], c[o + 5], c[o + 6], c[o + 7]};
      *(LAS f32x4*)ex = a; *(LAS f32x4*)(ex + 1024) = e;
    }
    float y[8] = {c0[0], c0[1], c0[2], c0[3], c0[4], c0[5], c0[6], c0[7]};
    lds_bar();
#pragma unroll
    for (int d = 0; d < 6; d++) { const f32x4 t = *(const LAS f32x4*)(L + wid * 6144 + d * 1024 + l16);
#pragma unroll
      for (int e = 0; e < 4; e++) y[(d & 1) * 4 + e] += t[e]; }
    const auto r0 = __builtin_amdgcn_permlane32_swap(cvtpk(y[0], y[1]), cvtpk(y[4], y[5]), false, false);
    const auto r1 = __builtin_amdgcn_permlane32_swap(cvtpk(y[2], y[3]), cvtpk(y[6], y[7]), false, false);
    const u32x4 yo = {r0[0], r1[0], r0[1], r1[1]};
    __builtin_amdgcn_raw_buffer_store_b128(yo, mkrsrc(out), orow + h2 * 16, 0, 0);
  }
}

NOPK __global__ __launch_bounds__(512, 4) void k2_kernel(const char* x, const char* w_sp, const char* b_sp, char* out, char* ws) {
  extern __shared__ __attribute__((aligned(16))) char lds[];
  if (__builtin_expect(BIDX() < 256, 1)) attn_body(ws, BIDX(), lds);
  else gemm_body(x, w_sp, b_sp, out, ws, BIDX() - 256, lds);
}
extern "C" void kernel_launch(void* const* d_in, const int* in_sizes, int n_in, void* d_out, int out_size, void* d_ws, size_t ws_size, hipStream_t stream) {
  (void)in_sizes; (void)n_in; (void)out_size; (void)ws_size;
  (void)hipFuncSetAttribute((const void*)k1_kernel, hipFuncAttributeMaxDynamicSharedMemorySize, K1_LDS);
  (void)hipFuncSetAttribute((const void*)k2_kernel, hipFuncAttributeMaxDynamicSharedMemorySize, K2_LDS);
  k1_kernel<<<256, 1024, K1_LDS, stream>>>((const char*)d_in[0], (const char*)d_in[1], (const char*)d_in[2], (const char*)d_in[4], (const char*)d_in[7], (char*)d_ws);
  k2_kernel<<<512, 512, K2_LDS, stream>>>((const char*)d_in[0], (const char*)d_in[5], (const char*)d_in[6], (char*)d_out, (char*)d_ws);
}
```
